```python
import torch

def _softmax(x, axis=-1):
    return torch.softmax(x, dim=axis)
def _gelu(x):
    return torch.nn.functional.gelu(x, approximate="tanh")
def _silu(x):
    return torch.nn.functional.silu(x)
def _sigmoid(x):
    return torch.sigmoid(x)
def _layernorm(x, axis=-1, eps=1e-5):
    m = x.mean(dim=axis, keepdim=True)
    v = x.var(dim=axis, keepdim=True, unbiased=False)
    return (x - m) / torch.sqrt(v + eps)
def _scatter_add(tgt, idx, src, axis=0):
    return tgt.clone().index_add_(axis, idx, src)
def _segment_sum(src, seg, n):
    out = torch.zeros((n,) + src.shape[1:], dtype=src.dtype)
    return out.index_add_(0, seg, src)
def _pad(x, pw):
    flat = tuple(v for pair in reversed(pw) for v in pair)
    return torch.nn.functional.pad(x, flat)

kernel_name = 'parallel_sum__ssm_conv_s4__dense_activation_gelu__prompt'

def setup_inputs():

    _w = (lambda a: (lambda t: t.to(torch.bfloat16) if t.is_floating_point() else t)(torch.as_tensor(a)))
    def _A_setup():
        B, L, D, N = 1, 2048, 512, 128
        A_re = -(lambda _x: torch.exp(torch.as_tensor(_x)))((lambda *s: torch.randn(*s, dtype=torch.bfloat16) * 12.8)(D, N))
        A_im = torch.pi * (lambda *s: torch.randn(*s, dtype=torch.bfloat16) * 0.05)(D, N)
        B_re = (lambda *s: torch.randn(*s, dtype=torch.bfloat16) * 0.1)(D, N)
        B_im = (lambda *s: torch.randn(*s, dtype=torch.bfloat16) * 0.1)(D, N)
        C_re = (lambda *s: torch.randn(*s, dtype=torch.bfloat16) * 0.2)(D, N)
        C_im = (lambda *s: torch.randn(*s, dtype=torch.bfloat16) * 0.2)(D, N)
        log_dt = (lambda *s: torch.randn(*s, dtype=torch.bfloat16) * 0.05)(D) * 0.5 - 4.0
        Dskip = (lambda *s: torch.randn(*s, dtype=torch.bfloat16) * 0.05)(D)
        return {
            "u": _w((lambda *s: torch.randn(*s, dtype=torch.bfloat16) * 0.05)(B, L, D)),
            "A_re": _w(A_re),
            "A_im": _w(A_im),
            "B_re": _w(B_re),
            "B_im": _w(B_im),
            "C_re": _w(C_re),
            "C_im": _w(C_im),
            "log_dt": _w(log_dt),
            "Dskip": _w(Dskip),
        }
    _A_in = _A_setup()

    def _B_setup():
        B, S, D, F = 1, 2048, 512, 1792
        return {
            "x": _w((lambda *s: torch.randn(*s, dtype=torch.bfloat16) * 0.05)(B, S, D)),
            "w1": _w((lambda *s: torch.randn(*s, dtype=torch.bfloat16) * 0.8)(D, F) * (D ** -0.5)),
            "b1": _w((lambda *s: torch.randn(*s, dtype=torch.bfloat16) * 12.8)(F) * 0.01),
            "w2": _w((lambda *s: torch.randn(*s, dtype=torch.bfloat16) * 0.8)(F, D) * (F ** -0.5)),
            "b2": _w((lambda *s: torch.randn(*s, dtype=torch.bfloat16) * 12.8)(D) * 0.01),
        }
    _B_in = _B_setup()

    return {
        "A_u": _A_in["u"],
        "A_A_re": _A_in["A_re"],
        "A_A_im": _A_in["A_im"],
        "A_B_re": _A_in["B_re"],
        "A_B_im": _A_in["B_im"],
        "A_C_re": _A_in["C_re"],
        "A_C_im": _A_in["C_im"],
        "A_log_dt": _A_in["log_dt"],
        "A_Dskip": _A_in["Dskip"],
        "B_x": _B_in["x"],
        "B_w1": _B_in["w1"],
        "B_b1": _B_in["b1"],
        "B_w2": _B_in["w2"],
        "B_b2": _B_in["b2"],
    }

def reference(A_u, A_A_re, A_A_im, A_B_re, A_B_im, A_C_re, A_C_im, A_log_dt, A_Dskip, B_x, B_w1, B_b1, B_w2, B_b2):
    def _A_ref(u, A_re, A_im, B_re, B_im, C_re, C_im, log_dt, Dskip):
        Bsz, L, D = u.shape
        dt = torch.exp(log_dt)[:, None]
        A = A_re + 1j * A_im
        Bc = B_re + 1j * B_im
        Cc = C_re + 1j * C_im
        logdA = dt * A
        dB = dt * Bc
        l = (lambda x, dt: x.to(dt))(torch.arange(L), A_re.dtype)
        P = torch.exp(logdA[:, :, None] * l[None, None, :])
        K = torch.einsum("dn,dnl,dn->dl", Cc, P, dB).real
        L2 = 2 * L
        Kp = _pad(K, ((0, 0), (0, L)))
        up = (lambda x, *d: x.permute(*d))(u, 0, 2, 1)
        up = _pad(up, ((0, 0), (0, 0), (0, L)))
        Kf = (lambda x, axes=(-2,-1): torch.fft.rfft2(x.float(), dim=axes))(Kp[:, None, :], axes=(-2, -1))
        Uf = (lambda x, axes=(-2,-1): torch.fft.rfft2(x.float(), dim=axes))(up[:, :, None, :], axes=(-2, -1))
        Yf = Uf * Kf[None, :, :, :]
        y = (lambda x, s, axes=(-2,-1): torch.fft.irfft2(x, s=s, dim=axes).to(torch.bfloat16))(Yf, (1, L2), axes=(-2, -1))[:, :, 0, :L]
        y = (lambda x, *d: x.permute(*d))(y, 0, 2, 1) + u * Dskip[None, None, :]
        return y

    def _B_ref(x, w1, b1, w2, b2):
        h = torch.einsum("bsd,df->bsf", x, w1) + b1
        h = _gelu(h)
        return torch.einsum("bsf,fd->bsd", h, w2) + b2

    x = A_u
    xn = _layernorm(x, axis=-1)
    return x + _A_ref(xn, A_A_re, A_A_im, A_B_re, A_B_im, A_C_re, A_C_im, A_log_dt, A_Dskip) + _B_ref(xn, B_w1, B_b1, B_w2, B_b2)
```

```cpp
#include <hip/hip_runtime.h>
#include <hip/hip_bf16.h>
#include <stdint.h>

typedef unsigned short u16;
__device__ __forceinline__ float bf2f(u16 b) { return __uint_as_float((unsigned)b << 16); }
__device__ __forceinline__ u16 f2bf(float f) { unsigned u = __float_as_uint(f); u += 0x7fffu + ((u >> 16) & 1u); return (u16)(u >> 16); }
__device__ __forceinline__ float rbf(float f) { return bf2f(f2bf(f)); }

constexpr int L = 2048, D = 512, N = 128, F = 1792;

__global__ void k_ln(const u16* x, u16* xn) {
  int row = blockIdx.x, t = threadIdx.x;
  float v[8]; float s = 0;
  for (int j = 0; j < 8; ++j) { v[j] = bf2f(x[row * D + t * 8 + j]); s += v[j]; }
  for (int o = 32; o; o >>= 1) s += __shfl_xor(s, o);
  float mf = s / D; float q = 0;
  for (int j = 0; j < 8; ++j) q += (v[j] - mf) * (v[j] - mf);
  for (int o = 32; o; o >>= 1) q += __shfl_xor(q, o);
  float m = rbf(mf), var = rbf(q / D);
  float sd = rbf(sqrtf(rbf(var + 1e-5f)));
  for (int j = 0; j < 8; ++j) xn[row * D + t * 8 + j] = f2bf(rbf(v[j] - m) / sd);
}
__global__ void k_kgen(const u16* Are, const u16* Aim, const u16* Bre, const u16* Bim, const u16* Cre, const u16* Cim, const u16* logdt, float* K) {
  int d = blockIdx.y; int l = blockIdx.x * 256 + threadIdx.x;
  float dt = rbf(expf(bf2f(logdt[d])));
  float lf = rbf((float)l);
  float acc = 0;
  for (int n = 0; n < N; ++n) {
    float lr = dt * bf2f(Are[d * N + n]), li = dt * bf2f(Aim[d * N + n]);
    float br = dt * bf2f(Bre[d * N + n]), bi = dt * bf2f(Bim[d * N + n]);
    float cr = bf2f(Cre[d * N + n]), ci = bf2f(Cim[d * N + n]);
    float wr = cr * br - ci * bi, wi = cr * bi + ci * br;
    float e = expf(lr * lf), s, c; sincosf(li * lf, &s, &c);
    acc += e * (wr * c - wi * s);
  }
  K[d * L + l] = acc;
}
__global__ void k_conv(const u16* xn, const float* K, const u16* Dskip, u16* ya) {
  int d = blockIdx.y; int l = blockIdx.x * 256 + threadIdx.x;
  __shared__ float ks[L], xs[L];
  for (int i = threadIdx.x; i < L; i += 256) { ks[i] = K[d * L + i]; xs[i] = bf2f(xn[i * D + d]); }
  __syncthreads();
  float acc = 0;
  for (int j = 0; j <= l; ++j) acc += ks[l - j] * xs[j];
  ya[l * D + d] = f2bf(rbf(acc) + rbf(xs[l] * bf2f(Dskip[d])));
}
__global__ void k_g1(const u16* xn, const u16* w1, const u16* b1, u16* h) {
  int l = blockIdx.y; int f = blockIdx.x * 256 + threadIdx.x;
  float acc = 0;
  for (int k = 0; k < D; ++k) acc += bf2f(xn[l * D + k]) * bf2f(w1[k * F + f]);
  float v = rbf(rbf(acc) + bf2f(b1[f]));
  float g = 0.5f * v * (1.f + tanhf(0.7978845608028654f * (v + 0.044715f * v * v * v)));
  h[l * F + f] = f2bf(g);
}
__global__ void k_g2(const u16* x, const u16* ya, const u16* h, const u16* w2, const u16* b2, u16* out) {
  int l = blockIdx.y; int d = blockIdx.x * 256 + threadIdx.x;
  float acc = 0;
  for (int k = 0; k < F; ++k) acc += bf2f(h[l * F + k]) * bf2f(w2[k * D + d]);
  float v = rbf(rbf(acc) + bf2f(b2[d]));
  out[l * D + d] = f2bf(rbf(bf2f(x[l * D + d]) + bf2f(ya[l * D + d])) + v);
}

extern "C" void kernel_launch(void* const* d_in, const int* in_sizes, int n_in,
                              void* d_out, int out_size, void* d_ws, size_t ws_size,
                              hipStream_t stream) {
  const u16* x = (const u16*)d_in[0];
  char* ws = (char*)d_ws;
  u16* xn = (u16*)ws; float* K = (float*)(ws + (4 << 20)); u16* ya = (u16*)(ws + (12 << 20)); u16* h = (u16*)(ws + (16 << 20));
  k_ln<<<L, 64, 0, stream>>>(x, xn);
  k_kgen<<<dim3(L / 256, D), 256, 0, stream>>>((const u16*)d_in[1], (const u16*)d_in[2], (const u16*)d_in[3], (const u16*)d_in[4], (const u16*)d_in[5], (const u16*)d_in[6], (const u16*)d_in[7], K);
  k_conv<<<dim3(L / 256, D), 256, 0, stream>>>(xn, K, (const u16*)d_in[8], ya);
  k_g1<<<dim3(F / 256, L), 256, 0, stream>>>(xn, (const u16*)d_in[10], (const u16*)d_in[11], h);
  k_g2<<<dim3(D / 256, L), 256, 0, stream>>>(x, ya, h, (const u16*)d_in[12], (const u16*)d_in[13], (u16*)d_out);
}
```

```cpp
#include <hip/hip_runtime.h>
#include <stdint.h>

#define ABL_LAST "7"
#define DI __device__ __forceinline__
#define LAS __attribute__((address_space(3)))
#define GAS __attribute__((address_space(1)))
#define CAS __attribute__((address_space(4)))
typedef unsigned short u16;
typedef unsigned long long u64;
typedef unsigned v4u __attribute__((ext_vector_type(4)));
typedef unsigned v2u __attribute__((ext_vector_type(2)));
typedef float f2 __attribute__((ext_vector_type(2)));
typedef float f4 __attribute__((ext_vector_type(4)));
typedef float f16v __attribute__((ext_vector_type(16)));
typedef __bf16 bf8 __attribute__((ext_vector_type(8)));
typedef __bf16 bf2 __attribute__((ext_vector_type(2)));
typedef short s4 __attribute__((ext_vector_type(4)));
typedef __amdgpu_buffer_rsrc_t rsrc_t;

constexpr int SL = 2048, DM = 512, NM = 128, FF = 1792;
constexpr int WS_E = 0, WS_F0 = 4096, WS_FH = 32768, WS_FY = 98304, WS_ST = 163840, WS_CS = 180224, WS_XT = 262144;
constexpr int WS_W2 = WS_XT + (2 << 20), WS_Y = WS_W2 + (2 << 20), WS_H = WS_Y + (2 << 20), WS_END = WS_H + SL * FF * 2;
constexpr int SA = 98304, TROW = 528, TAB = 59 * TROW;
constexpr int LN_OFF = SA + 40 * TROW, LN_ROW = 1088, W2S_OFF = SA + TAB + 40 * TROW;
constexpr int KC = 4160, KCH = 4 * KC, KK_OFF = 0, XI_OFF = 2 * KCH, XI = 4608, CX_OFF = XI_OFF + 2 * XI, YI_OFF = 131072;
constexpr int AP_OFF = 160640, BP_OFF = AP_OFF + 1024, CSP_OFF = BP_OFF + 1024, CNT_OFF = CSP_OFF + 1024, STP_OFF = CNT_OFF + 32, LDS_BYTES = 163840;
static_assert(SA + 2 * TAB <= AP_OFF && CX_OFF + 16384 <= 65536 && STP_OFF + 32 <= LDS_BYTES, "lds map");
constexpr int SC1 = 16;
constexpr int SPIN = 1 << 16;

DI float bf_lo(unsigned u) { return __builtin_bit_cast(float, u << 16); }
DI float bf_hi(unsigned u) { return __builtin_bit_cast(float, u & 0xffff0000u); }
DI unsigned pk(float a, float b) { f2 v = {a, b}; return __builtin_bit_cast(unsigned, __builtin_convertvector(v, bf2)); }
DI float rbf(float a) { return bf_lo(pk(a, a)); }
DI f16v mfma32(v4u a, v4u b, f16v c) { return __builtin_amdgcn_mfma_f32_32x32x16_bf16(__builtin_bit_cast(bf8, a), __builtin_bit_cast(bf8, b), c, 0, 0, 0); }
DI f4 mfma16(v4u a, v4u b, f4 c) { return __builtin_amdgcn_mfma_f32_16x16x32_bf16(__builtin_bit_cast(bf8, a), __builtin_bit_cast(bf8, b), c, 0, 0, 0); }
DI v2u trd(LAS char* p) { return __builtin_bit_cast(v2u, __builtin_amdgcn_ds_read_tr16_b64_v4i16((LAS s4*)p)); }
DI v4u cat(v2u a, v2u b) { return v4u{a.x, a.y, b.x, b.y}; }
#define LD128(p) (*(LAS v4u*)(p))
#define LD64(p) (*(LAS v2u*)(p))
#define LD32(p) (*(LAS unsigned*)(p))
#define LD16(p) (*(LAS u16*)(p))
#define SCHED() __builtin_amdgcn_sched_barrier(0)
#define BAR() do { SCHED(); asm volatile("" ::: "memory"); __builtin_amdgcn_s_waitcnt(0xC07F); asm volatile("s_barrier" ::: "memory"); SCHED(); } while (0)
#define LP(a) ((LAS char*)(size_t)(unsigned)(a))
#define VMW(n) asm volatile("s_waitcnt vmcnt(" #n ")" ::: "memory")
DI v4u mk_srd(const void* p, unsigned bytes) { u64 a = (u64)p; return v4u{(unsigned)a, (unsigned)(a >> 32) & 0xffffu, bytes, 0x00020000u}; }
DI rsrc_t mk_rsrc(const void* p, int bytes) { return __builtin_amdgcn_make_buffer_rsrc((void*)p, 0, bytes, 0x00020000); }
DI void dma(v4u srd, int voff, int soff, unsigned lds) {
  asm volatile("s_mov_b32 m0, %3\n\ts_nop 0\n\tbuffer_load_dwordx4 %0, %1, %2 offen lds" :: "v"(voff), "s"(srd), "s"(soff), "s"(lds) : "memory");
}
DI void dma_sc1(v4u srd, int voff, int soff, unsigned lds) {
  asm volatile("s_mov_b32 m0, %3\n\ts_nop 0\n\tbuffer_load_dwordx4 %0, %1, %2 offen sc1 lds" :: "v"(voff), "s"(srd), "s"(soff), "s"(lds) : "memory");
}
DI void dma4_sc1(v4u srd, int voff, int soff, unsigned lds) {
  asm volatile("s_mov_b32 m0, %3\n\ts_nop 0\n\tbuffer_load_dwordx4 %0, %1, %2 offen sc1 lds\n\tbuffer_load_dwordx4 %0, %1, %2 offen offset:1024 sc1 lds\n\t"
               "buffer_load_dwordx4 %0, %1, %2 offen offset:2048 sc1 lds\n\tbuffer_load_dwordx4 %0, %1, %2 offen offset:3072 sc1 lds"
               :: "v"(voff), "s"(srd), "s"(soff), "s"(lds) : "memory");
}
template <int C> DI float dpp_add(float v) { return v + __builtin_bit_cast(float, __builtin_amdgcn_update_dpp(0, __builtin_bit_cast(int, v), C, 0xf, 0xf, true)); }
DI float rl(float v, int l) { return __builtin_bit_cast(float, __builtin_amdgcn_readlane(__builtin_bit_cast(int, v), l)); }
DI float wave_sum(float v) {
  v = dpp_add<0xB1>(v); v = dpp_add<0x4E>(v); v = dpp_add<0x141>(v); v = dpp_add<0x140>(v);
  return (rl(v, 0) + rl(v, 16)) + (rl(v, 32) + rl(v, 48));
}
DI f2 cmul(f2 a, f2 b) { return f2{a.x * b.x - a.y * b.y, a.x * b.y + a.y * b.x}; }
DI f2 cexp_(float lr2, float lrev, float e) {
  float m = __builtin_amdgcn_exp2f(lr2 * e), a = __builtin_amdgcn_fractf(lrev * e);
  return f2{m * __builtin_amdgcn_cosf(a), m * __builtin_amdgcn_sinf(a)};
}
DI u64 ld64(char* ws, int off) { return __hip_atomic_load((GAS u64*)(ws + off), __ATOMIC_RELAXED, __HIP_MEMORY_SCOPE_AGENT); }
#define ORDER() __builtin_amdgcn_fence(__ATOMIC_ACQUIRE, "wavefront")
DI void st64(rsrc_t r, int off, unsigned lo, unsigned hi) { __builtin_amdgcn_raw_buffer_store_b64(v2u{lo, hi}, r, off, 0, SC1); }

#define STAMP(k) do {} while (0)
#define DET(v)
#define DETACC(a, b)

#define STB(k) do {} while (0)
#define P1UNROLL _Pragma("unroll 1")
#define LD64V(p) (*(volatile LAS v2u*)(p))
#define LD32V(p) (*(volatile LAS unsigned*)(p))
struct cz { float r, i, n; };
DI cz mkz(f2 z) { return cz{z.x, z.y, -z.y}; }
DI cz conj(cz z) { return cz{z.r, z.n, z.i}; }
DI f2 cm(f2 a, cz z) {
  f2 o;
  asm("v_mul_f32 %0, %2, %4\n\tv_mul_f32 %1, %2, %5\n\tv_fmac_f32 %0, %3, %6\n\tv_fmac_f32 %1, %3, %4" : "=&v"(o.x), "=&v"(o.y) : "v"(a.x), "v"(a.y), "v"(z.r), "v"(z.i), "v"(z.n));
  return o;
}
DI void step2(f2& v, cz zv, f2& u, cz zu, unsigned& pv, unsigned& pu) {
  f2 nv, nu;
  asm("v_mul_f32 %0, %6, %10\n\tv_mul_f32 %2, %8, %13\n\tv_mul_f32 %1, %6, %11\n\tv_mul_f32 %3, %8, %14\n\t"
      "v_cvt_pk_bf16_f32 %4, %6, %7\n\tv_fmac_f32 %0, %7, %12\n\tv_fmac_f32 %2, %9, %15\n\tv_cvt_pk_bf16_f32 %5, %8, %9\n\t"
      "v_fmac_f32 %1, %7, %10\n\tv_fmac_f32 %3, %9, %13"
      : "=&v"(nv.x), "=&v"(nv.y), "=&v"(nu.x), "=&v"(nu.y), "=&v"(pv), "=&v"(pu)
      : "v"(v.x), "v"(v.y), "v"(u.x), "v"(u.y), "v"(zv.r), "v"(zv.i), "v"(zv.n), "v"(zu.r), "v"(zu.i), "v"(zu.n));
  v = nv; u = nu;
}
DI bool tag_ok(u64 f, unsigned lo, unsigned hi) { return __all((unsigned)f == lo && (unsigned)(f >> 32) == hi); }

__global__ __launch_bounds__(512, 2) void fused_k(const u16* __restrict__ x, const u16* __restrict__ Are, const u16* __restrict__ Aim,
    const u16* __restrict__ Bre, const u16* __restrict__ Bim, const u16* __restrict__ Cre, const u16* __restrict__ Cim,
    const u16* __restrict__ logdt, const u16* __restrict__ Dskip, const u16* __restrict__ w1, const u16* __restrict__ b1,
    const u16* __restrict__ w2, const u16* __restrict__ b2, u16* __restrict__ out, char* __restrict__ ws) {
  extern __shared__ __attribute__((aligned(16))) char smem[];
  LAS char* const S = (LAS char*)smem;
  STAMP(0);
  const int tid = threadIdx.x, lane = tid & 63, wave = __builtin_amdgcn_readfirstlane(tid >> 6);
  const int b = blockIdx.x, xcd = b & 7, j = b >> 3, g8 = xcd * 32 + j;
  if (wave >= 4) __builtin_amdgcn_s_setprio(3);
  asm volatile("" :: "s"(x), "s"(Are), "s"(Aim), "s"(Bre), "s"(Bim), "s"(Cre), "s"(Cim), "s"(logdt), "s"(Dskip), "s"(w1), "s"(b1), "s"(w2), "s"(b2), "s"(out), "s"(ws));
  v4u xr;
  asm volatile("global_load_dwordx4 %0, %1, %2" : "=v"(xr) : "v"(g8 * 8192 + tid * 16), "s"(x) : "memory");
  const bool mainb = j < 28;
  const int pl = j >= 14 ? 1 : 0, pp = xcd * 2 + pl, fs = j - 14 * pl;
  const int lt = xcd * 4 + (j >> 3), dt = j & 7;
  const int ch0 = xcd * 64 + j * 2;
  const rsrc_t rw = mk_rsrc(ws, WS_END);
  const v4u sw_ = mk_srd(ws, WS_END);
  const int r31 = lane & 31, hh = lane >> 5, trq = (lane & 15) >> 2, trp = lane & 3, trb = (lane >> 4) & 1;
  const int q3 = wave & 3, dh = wave >> 2;
  v4u w2f[28];
  const int w2off = WS_W2 + ((2 * dt + dh) * 112 + 28 * q3) * 1024 + lane * 16;
  unsigned tlo, thi;
#define ROW_WAIT(n) float v_[8]; asm volatile("s_waitcnt vmcnt(" #n ")\n\tv_lshlrev_b32 %0, 16, %8\n\tv_and_b32 %1, 0xffff0000, %8\n\tv_lshlrev_b32 %2, 16, %9\n\tv_and_b32 %3, 0xffff0000, %9\n\t" \
    "v_lshlrev_b32 %4, 16, %10\n\tv_and_b32 %5, 0xffff0000, %10\n\tv_lshlrev_b32 %6, 16, %11\n\tv_and_b32 %7, 0xffff0000, %11" \
    : "=&v"(v_[0]), "=&v"(v_[1]), "=&v"(v_[2]), "=&v"(v_[3]), "=&v"(v_[4]), "=&v"(v_[5]), "=&v"(v_[6]), "=&v"(v_[7]) : "v"(xr[0]), "v"(xr[1]), "v"(xr[2]), "v"(xr[3]) : "memory")
  auto layernorm = [&](float (&v)[8]) {
    float s;
    s = ((v[0] + v[1]) + (v[2] + v[3])) + ((v[4] + v[5]) + (v[6] + v[7]));
    const float mf = wave_sum(s) * (1.f / DM);
    float q0 = 0.f, q1 = 0.f;
#pragma unroll
    for (int e = 0; e < 4; ++e) { float d = v[2 * e] - mf, f = v[2 * e + 1] - mf; q0 += d * d; q1 += f * f; }
    const float var = rbf(wave_sum(q0 + q1) * (1.f / DM));
    const float m = rbf(mf), sd = rbf(__builtin_amdgcn_sqrtf(rbf(var + 1e-5f))), rs = __builtin_amdgcn_rcpf(sd);
    v4u o;
#pragma unroll
    for (int e = 0; e < 4; ++e) { unsigned d = pk(v[2 * e] - m, v[2 * e + 1] - m); o[e] = pk(bf_lo(d) * rs, bf_hi(d) * rs); }
    LD128(S + LN_OFF + wave * LN_ROW + lane * 16) = o;
    if (lane == 0) LD32(S + STP_OFF + wave * 4) = pk(-m, sd);
  };
  const int hs = WS_H + (2 * lt * 112 + 28 * q3) * 1024, hl = q3 * 8192;

  if (wave < 4) {
    const int gw = wave, wf = gw & 1, wl = gw >> 1;
    if (mainb) {
      const v4u sx = mk_srd(x, SL * DM * 2), s1 = mk_srd(w1, DM * FF * 2);
      int vx[4], v1[4];
#pragma unroll
      for (int i = 0; i < 4; ++i) {
        vx[i] = (lane >> 3) * 1024 + (((lane & 7) ^ ((4 * (i & 1) + (lane >> 4)) & 7)) << 4) + i * 7168;
        v1[i] = (lane >> 4) * 3584 + (((lane & 15) ^ ((lane >> 4) << 2) ^ i) << 4) + i * 13312;
      }
      const int rot = (fs + 4 * pl + xcd) & 7;
      const int sx0 = (pp * 128 + 32 * gw) * 1024, s10 = 16 * gw * 3584 + fs * 256;
      auto issue8 = [&](int s) {
        const int ks = (s + rot) & 7;
        asm volatile("s_mov_b32 m0, %12\n\ts_nop 0\n\tbuffer_load_dwordx4 %0, %8, %10 offen lds\n\tbuffer_load_dwordx4 %1, %8, %10 offen offset:1024 lds\n\t"
                     "buffer_load_dwordx4 %2, %8, %10 offen offset:2048 lds\n\tbuffer_load_dwordx4 %3, %8, %10 offen offset:3072 lds\n\t"
                     "s_add_u32 m0, m0, 0x4000\n\ts_nop 0\n\tbuffer_load_dwordx4 %4, %9, %11 offen lds\n\tbuffer_load_dwordx4 %5, %9, %11 offen offset:1024 lds\n\t"
                     "buffer_load_dwordx4 %6, %9, %11 offen offset:2048 lds\n\tbuffer_load_dwordx4 %7, %9, %11 offen offset:3072 lds"
                     :: "v"(vx[0]), "v"(vx[1]), "v"(vx[2]), "v"(vx[3]), "v"(v1[0]), "v"(v1[1]), "v"(v1[2]), "v"(v1[3]), "s"(sx), "s"(s1),
                        "s"(sx0 + ks * 128), "s"(s10 + ks * 229376), "s"(s * 32768 + gw * 4096) : "memory", "scc");
      };
      issue8(0); issue8(1);
      SCHED();
      STAMP(13);
      f16v acc[2][2];
#pragma unroll
      for (int a = 0; a < 2; ++a)
#pragma unroll
        for (int c = 0; c < 2; ++c) {
#pragma unroll
          for (int i = 0; i < 16; ++i) acc[a][c][i] = 0.f;
          asm volatile("" : "+v"(acc[a][c]));
        }
      int xa[4], wa[4];
#pragma unroll
      for (int i = 0; i < 4; ++i) {
        const int ft = i >> 1, t = i & 1;
        xa[i] = (64 * wl + r31) * 128 + (((2 * i + hh) ^ ((r31 >> 1) & 7)) << 4);
        wa[i] = (8 * hh + 4 * t + trq) * 256 + (((4 * (2 * wf + ft) + 2 * trb + (trp >> 1)) ^ ((trq << 2) | ((2 * hh + t) & 3))) << 4) + 8 * (trp & 1);
        asm volatile("" : "+v"(xa[i]), "+v"(wa[i]));
      }
      ROW_WAIT(16);
      layernorm(v_);
      SCHED();
      issue8(2);
      v2u FA[2][4][4]; v4u FB[2][4][2];
      auto rd = [&](int set, int n, bool first = false) {
        const int ks = n / 6, i = n % 6;
        if (i == 2 || i == 3) FB[set][ks][i - 2] = LD128(LP(xa[ks]) + (i - 2) * 4096);
        else { const int k = i < 2 ? i : i - 2; FA[set][ks][k] = trd(LP(wa[k]) + 16384 + ks * 4096); }
      };
      VMW(16);
      STAMP(7);
      STB(1);
      BAR();
      STAMP(1);
#pragma unroll
      for (int n = 0; n < 24; ++n) rd(0, n, true);
      int slot = 0;
      auto stage = [&](int set, int s) {
        const int dsl = slot == 65536 ? -65536 : 32768;
        const int ks = (s + 3 + rot) & 7, so_x = sx0 + ks * 128, so_w = s10 + ks * 229376, m0v = slot + gw * 4096;
        STB(2 + s);
        DET(d0_);
        asm volatile("s_cmp_lt_i32 %0, 6\n\ts_cbranch_scc1 1f\n\ts_waitcnt vmcnt(0)\n1:\n\ts_waitcnt vmcnt(8)" :: "s"(s) : "memory", "scc");
        DET(d1_);
        BAR();
        DET(d2_);
        DETACC(5, 6);
#pragma unroll
        for (int n = 0; n < 16; ++n) {
          const int k = n >> 2, a = (n >> 1) & 1, c = n & 1;
          acc[a][c] = mfma32(cat(FA[set][k][2 * a], FA[set][k][2 * a + 1]), FB[set][k][c], acc[a][c]);
          SCHED();
          if (n == 0) {
#pragma unroll
            for (int i = 0; i < 4; ++i) wa[i] += dsl;
            xa[0] += dsl;
          }
          if (n >= 1 && n <= 12) { rd(set ^ 1, 2 * n - 2); rd(set ^ 1, 2 * n - 1); }
          if (n == 1 || n == 4 || n == 7) xa[n / 3 + 1] += dsl;
          if (n >= 8) {
            const int i = n & 3;
            if (n == 8) asm volatile("s_cmp_gt_i32 %4, " ABL_LAST "\n\ts_cbranch_scc1 1f\n\ts_mov_b32 m0, %3\n\ts_nop 0\n\tbuffer_load_dwordx4 %0, %1, %2 offen lds\n1:" :: "v"(vx[0]), "s"(sx), "s"(so_x), "s"(m0v), "s"(s + 3) : "memory", "scc");
            else if (n == 12) asm volatile("s_cmp_gt_i32 %4, " ABL_LAST "\n\ts_cbranch_scc1 1f\n\ts_mov_b32 m0, %3\n\ts_nop 0\n\tbuffer_load_dwordx4 %0, %1, %2 offen lds\n1:" :: "v"(v1[0]), "s"(s1), "s"(so_w), "s"(m0v + 16384), "s"(s + 3) : "memory", "scc");
            else asm volatile("s_cmp_gt_i32 %4, " ABL_LAST "\n\ts_cbranch_scc1 1f\n\tbuffer_load_dwordx4 %0, %1, %2 offen offset:%3 lds\n1:" :: "v"(n < 12 ? vx[i] : v1[i]), "s"(n < 12 ? sx : s1), "s"(n < 12 ? so_x : so_w), "i"(i * 1024), "s"(s + 3) : "memory", "scc");
          }
          SCHED();
        }
        slot += dsl;
      };
P1UNROLL
      for (int s = 0; s < 8; s += 2) { stage(0, s); stage(1, s + 1); }
      STB(10);
      STAMP(2);
      v4u Ap[2], Bp[2]; float sd[2];
#pragma unroll
      for (int i = 0; i < 2; ++i) {
        v2u a = LD64(S + AP_OFF + (64 * wf + 32 * i + r31) * 8), bb = LD64(S + BP_OFF + (64 * wl + 32 * i + r31) * 8);
        sd[i] = bf_hi(bb.y);
        Ap[i] = v4u{hh ? 0u : a.x, hh ? 0u : a.y, 0u, 0u};
        Bp[i] = v4u{hh ? 0u : bb.x, hh ? 0u : bb.y, 0u, 0u};
      }
#pragma unroll
      for (int c = 0; c < 2; ++c)
#pragma unroll
        for (int a = 0; a < 2; ++a) acc[a][c] = mfma32(Ap[a], Bp[c], acc[a][c]);
      STAMP(3);
#pragma unroll
      for (int c = 0; c < 2; ++c) {
        const float s = sd[c], rs = __builtin_amdgcn_rcpf(s);
        const float c0 = -2.3022081985f * rs, c1 = c0 * 0.044715f * rs * rs;
        const f2 c0v = {c0, c0}, c1v = {c1, c1}, sv = {s, s};
#pragma unroll
        for (int a = 0; a < 2; ++a)
#pragma unroll
          for (int u = 0; u < 2; ++u) {
            v4u o;
#pragma unroll
            for (int e = 0; e < 4; ++e) {
              f2 v = {acc[a][c][8 * u + 2 * e], acc[a][c][8 * u + 2 * e + 1]};
              f2 z = v * __builtin_elementwise_fma(v * v, c1v, c0v);
              f2 ex = {__builtin_amdgcn_exp2f(z.x), __builtin_amdgcn_exp2f(z.y)};
              f2 d = __builtin_elementwise_fma(ex, sv, sv);
              f2 r = {__builtin_amdgcn_rcpf(d.x), __builtin_amdgcn_rcpf(d.y)};
              f2 g = v * r;
              o[e] = pk(g.x, g.y);
            }
            __builtin_amdgcn_raw_buffer_store_b128(o, rw, lane * 16 + (WS_H + ((4 * pp + 2 * wl + c) * 112 + 8 * fs + 4 * wf + 2 * a + u) * 1024), 0, SC1);
          }
      }
    } else {
      const int sp = min(xcd * 4 + (j - 28), 27);
      float cs[8];
#pragma unroll
      for (int i = 0; i < 8; ++i) cs[i] = 0.f;
      const GAS char* p = (const GAS char*)w1 + (size_t)(gw * 128 + (lane >> 3)) * 3584 + sp * 128 + (lane & 7) * 16;
      v4u v[16];
#pragma unroll
      for (int i = 0; i < 16; ++i) v[i] = *(const GAS v4u*)(p + i * 8 * 3584);
      SCHED();
      ROW_WAIT(16);
      layernorm(v_);
      BAR();
      STAMP(1);
#pragma unroll
      for (int i = 0; i < 16; ++i)
#pragma unroll
        for (int e = 0; e < 4; ++e) { cs[2 * e] += bf_lo(v[i][e]); cs[2 * e + 1] += bf_hi(v[i][e]); }
#pragma unroll
      for (int i = 0; i < 8; ++i) { cs[i] += __shfl_xor(cs[i], 8); cs[i] += __shfl_xor(cs[i], 16); cs[i] += __shfl_xor(cs[i], 32); }
      if (lane < 8) {
        *(LAS f4*)(S + CSP_OFF + gw * 256 + lane * 32) = f4{cs[0], cs[1], cs[2], cs[3]};
        *(LAS f4*)(S + CSP_OFF + gw * 256 + lane * 32 + 16) = f4{cs[4], cs[5], cs[6], cs[7]};
      }
      BAR();
      if (xcd * 4 + (j - 28) < 28 && gw == 0) {
        const unsigned tl = LD32(S + CNT_OFF + 8);
        LAS float* c = (LAS float*)(S + CSP_OFF);
        float t = (c[lane] + c[64 + lane]) + (c[128 + lane] + c[192 + lane]);
        st64(rw, WS_CS + (sp * 64 + lane) * 8, __builtin_bit_cast(unsigned, t), tl);
      }
      STAMP(2);
#pragma unroll 1
      for (int s = 0; s < 7; ++s) BAR();
    }
    SCHED();
    if (lane == 0) LD32V(S + CNT_OFF + 16 + gw * 4) = 1u;
    { v2u t = LD64(S + CNT_OFF + 8); tlo = __builtin_amdgcn_readfirstlane(t.x); thi = __builtin_amdgcn_readfirstlane(t.y); }
    STAMP(4);
#pragma unroll 1
    for (int sp = 0; sp < SPIN && LD32V(S + CNT_OFF) < 4u; ++sp) __builtin_amdgcn_s_sleep(1);
    asm volatile("" ::: "memory");
    SCHED();
#pragma unroll
    for (int i = 0; i < 14; ++i) w2f[i] = __builtin_amdgcn_raw_buffer_load_b128(rw, w2off + i * 1024, 0, SC1);
    SCHED();
    VMW(14);
    if (mainb && lane == 0) st64(rw, WS_FH + (((pp * 2 + wl) * 14 + fs) * 2 + wf) * 64, tlo, thi);
    STAMP(5);
    SCHED();
#pragma unroll
    for (int i = 14; i < 28; ++i) w2f[i] = __builtin_amdgcn_raw_buffer_load_b128(rw, w2off + i * 1024, 0, SC1);
    STAMP(6);
    {
      const int fo = WS_FH + (lt * 28 + 7 * q3 + min(lane, 6)) * 64;
#pragma unroll 1
      for (int it = 0; it < SPIN; ++it) if (tag_ok(ld64(ws, fo), tlo, thi)) break;
    }
    STAMP(9);
#pragma unroll
    for (int t = 0; t < 2; ++t)
#pragma unroll
      for (int c = 0; c < 2; ++c) dma4_sc1(sw_, lane * 16, hs + c * 114688 + t * 4096, (t + 2) * 32768 + hl + c * 4096);
    BAR();
#pragma unroll
    for (int c = 0; c < 2; ++c) dma4_sc1(sw_, lane * 16, hs + c * 114688 + 2 * 4096, hl + c * 4096);
    VMW(16);
  } else {
    const int sw = wave - 4, cc = sw >> 1, sh = sw & 1, ch = ch0 + cc;
    const v2u ev = __builtin_amdgcn_raw_buffer_load_b64(rw, WS_E, 0, SC1);
    const int pi = ch * NM + 64 * sh + lane;
    const u16 p_ar = Are[pi], p_ai = Aim[pi], p_br = Bre[pi], p_bi = Bim[pi], p_cr = Cre[pi], p_ci = Cim[pi];
    SCHED();
    const int pid = (xcd * 28 + min(j, 27)) * 4 + sw;
    {
      const v4u s2 = mk_srd(w2, FF * DM * 2);
      const int so = (pid >> 3) * 16 * 1024 + (pid & 7) * 128, vo = (lane >> 3) * 1024 + (lane & 7) * 16;
      dma(s2, vo, so, W2S_OFF + sw * 2048);
      dma(s2, vo, so + 8192, W2S_OFF + sw * 2048 + 1024);
    }
    unsigned codew;
    {
      u64 pc; unsigned csz;
      asm volatile("s_getpc_b64 %0\n.Lfk_begin:\n\ts_mov_b32 %1, .Lfk_end-.Lfk_begin-4" : "=s"(pc), "=s"(csz));
      unsigned o = min((unsigned)(j * 8 + sw * 2 + (lane & 1)) * 128u, csz);
      asm volatile("global_load_dword %0, %1, %2" : "=v"(codew) : "v"(o), "s"(pc) : "memory");
    }
    __builtin_amdgcn_s_setprio(0);
    const unsigned ldt2 = *(const CAS unsigned*)(logdt + ch0), dsk2 = *(const CAS unsigned*)(Dskip + ch0);
    const float ldt = cc ? bf_hi(ldt2) : bf_lo(ldt2), dsk = cc ? bf_hi(dsk2) : bf_lo(dsk2);
    ROW_WAIT(10);
    STAMP(13);
    layernorm(v_);
    { u64 e = (((u64)ev.y << 32) | ev.x) + 1; tlo = __builtin_amdgcn_readfirstlane((unsigned)e); thi = __builtin_amdgcn_readfirstlane((unsigned)(e >> 32)); }
    if (tid == 256) { *(LAS v4u*)(S + CNT_OFF) = v4u{0u, 0u, tlo, thi}; *(LAS v4u*)(S + CNT_OFF + 16) = v4u{0u, 0u, 0u, 0u}; }
    STAMP(1);
    STB(1);
    BAR();
    VMW(0);
    asm volatile("" :: "v"(codew));
    if (tid >= 504) st64(rw, WS_ST + (g8 * 8 + (lane & 7)) * 8, LD32(S + STP_OFF + (lane & 7) * 4), tlo);
#pragma unroll
    for (int u = 0; u < 2; ++u) {
      LAS char* p = S + LN_OFF + trq * LN_ROW + (128 * sw + 64 * u + (lane & 48) + 4 * trp) * 2;
      v4u o = cat(trd(p), trd(p + 4 * LN_ROW));
      __builtin_amdgcn_raw_buffer_store_b128(o, rw, lane * 16 + (WS_XT + g8 * 8192 + (128 * sw + 64 * u) * 16), 0, SC1);
    }
    if (mainb) {
#pragma unroll
      for (int u = 0; u < 2; ++u) {
        LAS char* p = S + W2S_OFF + sw * 2048 + (4 * hh + trq) * 128 + (32 * u + 16 * trb + 4 * trp) * 2;
        v4u o = cat(trd(p), trd(p + 1024));
        __builtin_amdgcn_raw_buffer_store_b128(o, rw, lane * 16 + (WS_W2 + ((2 * (pid & 7) + u) * 112 + (pid >> 3)) * 1024), 0, SC1);
      }
    }
    STAMP(2);
    STB(2);
    BAR();
    const float dtv = rbf(__builtin_amdgcn_exp2f(ldt * 1.4426950408889634f));
    float lr2 = (dtv * bf_lo(p_ar)) * 1.4426950408889634f, lrev = (dtv * bf_lo(p_ai)) * 0.15915494309189535f;
    asm("" : "+v"(lr2));
    float dbr = dtv * bf_lo(p_br);
    asm("" : "+v"(dbr));
    const f2 cb = cm(f2{bf_lo(p_cr), -bf_lo(p_ci)}, conj(mkz(f2{dbr, dtv * bf_lo(p_bi)})));
    const cz z8 = mkz(cexp_(lr2, lrev, 8.f)), z8c = conj(z8), z1 = conj(mkz(cexp_(lr2, lrev, 1.f))), z256 = conj(mkz(cexp_(lr2, lrev, 256.f)));
    const cz z2 = mkz(cm(f2{z1.r, z1.i}, z1)), z4 = mkz(cm(f2{z2.r, z2.i}, z2));
    LAS char* const T = S + SA + cc * TAB + (64 * sh + lane) * 4;
    f2 V = {1.f, 0.f}, U = cb, W = cb;
    auto put = [&](int r, cz zu) {
 unsigned pv, pu; step2(V, z8, U, zu, pv, pu); LD32V(T + r * TROW) = pv; LD32V(T + (32 + r) * TROW) = pu; };
    STB(3);
    BAR();
#pragma unroll
    for (int i = 0; i < 6; ++i) put(i, z1);
    STB(4);
    BAR();
    put(6, z1); put(7, z1);
    W = cm(W, z256); U = W;
#pragma unroll
    for (int i = 0; i < 4; ++i) put(8 + i, z2);
    VMW(0);
    STB(5);
    BAR();
    if (tid == 256) st64(rw, WS_F0 + b * 64, tlo, thi);
    put(12, z2);
    W = cm(W, z256); U = W;
    put(13, z4); put(14, z4); put(15, z4);
    W = cm(W, z256); U = W;
    put(16, z4); put(17, z4);
    STB(6);
    BAR();
    const int goff = cc == 0 ? WS_ST + (pp * 128 + 64 * sh + lane) * 8 : WS_CS + (min(fs, 13) * 128 + 64 * sh + lane) * 8;
    const unsigned b1v = b1[min(fs, 13) * 128 + 64 * sh + lane];
    u64 gr = ld64(ws, goff);
    put(18, z4);
#pragma unroll
    for (int k = 0; k < 2; ++k) { W = cm(W, z256); U = W; put(19 + 2 * k, z8c); put(20 + 2 * k, z8c); }
    STB(7);
    BAR();
    const int pg = 64 * sw + lane;
    const int foff = WS_F0 + (((pg & 31) << 3) | (pg >> 5)) * 64;
    u64 f0 = ld64(ws, foff);
#pragma unroll
    for (int k = 2; k < 4; ++k) { W = cm(W, z256); U = W; put(19 + 2 * k, z8c); put(20 + 2 * k, z8c); }
    STB(8);
    BAR();
#pragma unroll
    for (int i = 27; i < 32; ++i) { LD32V(T + i * TROW) = pk(V.x, V.y); V = cm(V, z8); }
    if (mainb) {
#pragma unroll 1
      for (int sp = 0; sp < SPIN && !__all((unsigned)(gr >> 32) == tlo); ++sp) gr = ld64(ws, goff);
      const unsigned pv = (unsigned)gr;
      if (cc == 0) {
        const unsigned nm = pv & 0xffffu;
        LD64(S + BP_OFF + (64 * sh + lane) * 8) = v2u{nm | (nm << 16), pv};
      } else {
        const float c = __builtin_bit_cast(float, pv), c_hi = rbf(c), r1 = c - c_hi, c_mid = rbf(r1), r2 = r1 - c_mid;
        LD64(S + AP_OFF + (64 * sh + lane) * 8) = v2u{pk(c_hi, c_mid), (pk(r2, r2) & 0xffffu) | (b1v << 16)};
      }
    }
    STAMP(3);
    STB(9);
    BAR();
#pragma unroll 1
    for (int it = 0; it < SPIN && !tag_ok(f0, tlo, thi); ++it) f0 = ld64(ws, foff);
    ORDER();
    if (lane == 0) __hip_atomic_fetch_add((LAS unsigned*)(S + CNT_OFF + 4), 1u, __ATOMIC_RELAXED, __HIP_MEMORY_SCOPE_WORKGROUP);
    v4u xv[2];
#pragma unroll
    for (int u = 0; u < 2; ++u) xv[u] = __builtin_amdgcn_raw_buffer_load_b128(rw, (lane >> 1) * 8192 + (lane & 1) * 16, WS_XT + (64 * sw + 32 * u) * 8192 + ch0 * 16, SC1);
    auto ldw = [&](int i) { w2f[i] = __builtin_amdgcn_raw_buffer_load_b128(rw, w2off + (i & 3) * 1024, (i >> 2) * 4096, SC1); };
    f16v kk;
    {
      const int t = r31 & 7, bk = r31 >> 3;
      int ur;
      if (sh) ur = 19 + 2 * bk + (t >= 5 ? 1 : 0);
      else ur = bk == 0 ? t : (bk == 1 ? 8 + ((0x43222100u >> (4 * t)) & 15) : 7 + 3 * bk + ((0x22111000u >> (4 * t)) & 15));
      LAS char* pa = S + SA + cc * TAB + (32 + ur) * TROW + hh * 16;
      LAS char* pb = S + SA + cc * TAB + r31 * TROW + hh * 16;
#pragma unroll
      for (int i = 0; i < 16; ++i) kk[i] = 0.f;
      v4u A[16], B[16];
#pragma unroll
      for (int s = 0; s < 6; ++s) { A[s] = LD128(pa + s * 32); B[s] = LD128(pb + s * 32); }
#pragma unroll 1
      for (int sp = 0; sp < SPIN && LD32V(S + CNT_OFF + 4) < 4u; ++sp) __builtin_amdgcn_s_sleep(1);
      asm volatile("" ::: "memory");
      if (b == 0 && tid == 256) st64(rw, WS_E, tlo, thi);
      STAMP(4);
      SCHED();
#pragma unroll
      for (int s = 0; s < 16; ++s) {
        if (s + 6 < 16) { A[s + 6] = LD128(pa + (s + 6) * 32); B[s + 6] = LD128(pb + (s + 6) * 32); }
        kk = mfma32(A[s], B[s], kk);
        if (s < 12) ldw(s);
        SCHED();
      }
    }
    STAMP(5);
    {
      LAS char* kb = S + KK_OFF + cc * KCH + 8;
#pragma unroll
      for (int g = 0; g < 4; ++g) {
        float k0 = kk[4 * g], k1 = kk[4 * g + 1], k2 = kk[4 * g + 2], k3 = kk[4 * g + 3];
        if (sh && hh && (r31 & 1)) k0 = k1;
        const unsigned e01 = pk(k3, k2), e23 = pk(k1, k0), e12 = __builtin_amdgcn_alignbit(e23, e01, 16);
        LAS char* p = kb + (2044 - (256 * (4 * sh + 3) + 8 * r31 + 4 * hh)) * 2 + (3 - g) * 512;
        LD64(p) = v2u{e01, e23};
        LD16(p + KC - 2) = (u16)e01; LD32V(p + KC) = e12; LD16(p + KC + 4) = (u16)(e23 >> 16);
        LD32V(p + 2 * KC - 4) = e01; LD32V(p + 2 * KC) = e23;
        LD16(p + 3 * KC - 6) = (u16)e01; LD32V(p + 3 * KC - 4) = e12; LD16(p + 3 * KC) = (u16)(e23 >> 16);
        ldw(12 + 2 * g); ldw(13 + 2 * g);
        SCHED();
      }
      if (sh == 0) {
        const int s = lane >> 4, i = lane & 15;
        LD16(kb + s * KC + (2048 - s + i) * 2) = 0;
      } else if (lane < 30) {
        LD128(S + XI_OFF + cc * XI + lane * 16) = v4u{0u, 0u, 0u, 0u};
      }
#pragma unroll
      for (int u = 0; u < 2; ++u) LD128(S + XI_OFF + (lane & 1) * XI + 480 + (64 * sw + 32 * u + (lane >> 1)) * 16) = xv[u];
      asm volatile("s_waitcnt lgkmcnt(0)" ::: "memory");
      if (lane == 0) __hip_atomic_fetch_add((LAS unsigned*)(S + CNT_OFF), 1u, __ATOMIC_RELAXED, __HIP_MEMORY_SCOPE_WORKGROUP);
#pragma unroll
      for (int i = 20; i < 28; ++i) ldw(i);
      SCHED();
    }
#pragma unroll 1
    for (int sp = 0; sp < SPIN && LD32V(S + CNT_OFF) < 4u; ++sp) __builtin_amdgcn_s_sleep(1);
    asm volatile("" ::: "memory");
    STAMP(6);
    f4 ya[8];
#pragma unroll
    for (int i = 0; i < 8; ++i) ya[i] = f4{0.f, 0.f, 0.f, 0.f};
    {
      const int m = lane & 15, g = lane >> 4;
      LAS char* pa = S + KK_OFF + cc * KCH + (3 - (m & 3)) * KC + 8 + (2044 - 16 * (16 * 7 + 2 * (4 * sh + 3) + 1) - 4 * (m >> 2) + 8 * g) * 2;
      LAS char* pb = S + XI_OFF + cc * XI + (30 + 2 * m - 4 * (4 * sh + 3) - 2 + g) * 16;
      v4u A[4][8], B[4][8];
      auto lda = [&](int q, int p) { LAS char* a = pa + 512 * (7 - p) + 64 * (3 - q); A[q][p] = cat(LD64V(a), LD64V(a + 8)); };
      auto ldb = [&](int q, int e) { B[q][e] = *(volatile LAS v4u*)(pb + 512 * e + 64 * (3 - q)); };
      lda(0, 0);
#pragma unroll
      for (int e = 7; e >= 0; --e) ldb(0, e);
      lda(0, 1); lda(0, 2);
#pragma unroll 1
      for (int sp = 0; sp < SPIN && LD32V(S + CNT_OFF + 16 + sw * 4) == 0u; ++sp) __builtin_amdgcn_s_sleep(1);
      STAMP(9);
      SCHED();
#pragma unroll
      for (int q = 0; q < 4; ++q) {
        int n = 0;
#pragma unroll
        for (int p = 0; p < 8; ++p)
#pragma unroll
          for (int e = 7 - p; e >= 0; --e) {
            ya[p + e] = mfma16(A[q][p], B[q][e], ya[p + e]);
            if (n == 2) lda(q, 3);
            if (n == 8) lda(q, 4);
            if (n == 12) lda(q, 5);
            if (n == 16) lda(q, 6);
            if (n == 18) lda(q, 7);
            if (q < 3) {
              if (n == 20) lda(q + 1, 0);
              if (n == 28) lda(q + 1, 1);
              if (n == 34) lda(q + 1, 2);
              if (n == 4) ldb(q + 1, 7);
              if (n == 10) ldb(q + 1, 6);
              if (n == 17) ldb(q + 1, 5);
              if (n == 22) ldb(q + 1, 4);
              if (n == 24) ldb(q + 1, 3);
              if (n == 26) ldb(q + 1, 2);
              if (n == 30) ldb(q + 1, 1);
              if (n == 31) ldb(q + 1, 0);
            }
            SCHED();
            ++n;
          }
      }
    }
    f4 own[4];
#pragma unroll
    for (int i = 0; i < 4; ++i) {
      *(LAS f4*)(S + CX_OFF + sw * 4096 + i * 1024 + lane * 16) = sh ? ya[i] : ya[4 + i];
      own[i] = sh ? ya[4 + i] : ya[i];
    }
    STAMP(7);
    BAR();
    __builtin_amdgcn_s_waitcnt(0x0F70);
#pragma unroll
    for (int i = 0; i < 4; ++i) {
      const f4 y = own[i] + *(LAS f4*)(S + CX_OFF + (sw ^ 1) * 4096 + i * 1024 + lane * 16);
      const int l = 256 * (4 * sh + i) + 16 * (lane & 15) + 4 * (lane >> 4);
      const v2u xq = LD64(S + XI_OFF + cc * XI + 480 + l * 2);
      const unsigned y01 = pk(y[0], y[1]), y23 = pk(y[2], y[3]);
      const unsigned d01 = pk(bf_lo(xq.x) * dsk, bf_hi(xq.x) * dsk), d23 = pk(bf_lo(xq.y) * dsk, bf_hi(xq.y) * dsk);
      st64(rw, WS_Y + ch * 4096 + l * 2, pk(bf_lo(y01) + bf_lo(d01), bf_hi(y01) + bf_hi(d01)), pk(bf_lo(y23) + bf_lo(d23), bf_hi(y23) + bf_hi(d23)));
    }
  }
  STAMP(8);
  BAR();
  STAMP(10);
  const int hso = hs + dh * 114688, hlo = hl + dh * 4096;
  LAS char* const hp = S + hl + lane * 16;
  LAS char* hp2 = hp + 65536;
  asm volatile("" : "+v"(hp2));
  v4u H[2][8];
  auto ldh = [&](int t, int i) { const int sl = (t + 2) & 3; H[t & 1][i] = LD128((sl & 2 ? hp2 : hp) + (sl & 1) * 32768 + i * 1024); };
#pragma unroll
  for (int i = 0; i < 4; ++i) { ldh(0, i); ldh(0, 4 + i); }
  dma4_sc1(sw_, lane * 16, hso + 3 * 4096, 32768 + hlo);
  f16v acc[2];
#pragma unroll
  for (int c = 0; c < 2; ++c)
#pragma unroll
    for (int i = 0; i < 16; ++i) acc[c][i] = 0.f;
  const int dl = 32 * dh + 16 * (q3 & 1), ll = 32 * (q3 >> 1);
  const int fyo = WS_FY + ((lt >> 4) * 512 + 64 * dt + 8 * wave + (lane & 7)) * 64;
  u64 fy = 0;
  v2u xo[2], bo[2];
#pragma unroll
  for (int t = 0; t < 7; ++t) {
    if (t < 6) {
      DET(d0_);
      if (t == 0) VMW(12); else if (t < 4) VMW(8); else if (t == 4) VMW(4); else VMW(0);
      DET(d1_);
      BAR();
      DET(d2_);
      DETACC(3, 4);
      if (t == 2 && wave >= 4 && lane == 0) st64(rw, WS_FY + (((wave & 1) * 512) + ch0 + ((wave >> 1) & 1)) * 64, tlo, thi);
    }
    if (t == 5) {
#pragma unroll 1
      for (int it = 0; it < SPIN && !tag_ok(fy, tlo, thi); ++it) fy = ld64(ws, fyo);
      dma_sc1(sw_, (lane >> 3) * 4096 + (lane & 7) * 16, WS_Y + (64 * dt + 8 * wave) * 4096 + lt * 128, YI_OFF + wave * 1024);
    }
    SCHED();
#pragma unroll
    for (int i = 0; i < 4; ++i) {
      acc[0] = mfma32(w2f[4 * t + i], H[t & 1][i], acc[0]);
      SCHED();
      if (t < 6 && i < 2) { ldh(t + 1, 2 * i); ldh(t + 1, 4 + 2 * i); }
      SCHED();
      acc[1] = mfma32(w2f[4 * t + i], H[t & 1][4 + i], acc[1]);
      SCHED();
      if (t < 6 && i < 2) { ldh(t + 1, 2 * i + 1); ldh(t + 1, 5 + 2 * i); }
      SCHED();
      if (i == 2 && t <= 2) dma4_sc1(sw_, lane * 16, hso + (t + 4) * 4096, ((t + 2) & 3) * 32768 + hlo);
      if (i == 2 && t == 4) {
        fy = ld64(ws, fyo);
#pragma unroll
        for (int u = 0; u < 2; ++u) {
          xo[u] = *(const GAS v2u*)((const GAS char*)x + (size_t)(64 * lt + ll + r31) * 1024 + (64 * dt + dl + 8 * u + 4 * hh) * 2);
          bo[u] = *(const GAS v2u*)((const GAS char*)b2 + (64 * dt + dl + 8 * u + 4 * hh) * 2);
        }
      }
      SCHED();
    }
  }
  STAMP(11);
  float o8[8];
#pragma unroll
  for (int i = 0; i < 4; ++i) {
    const f16v& a = acc[i >> 1];
    const int r0 = 8 * (i & 1);
    if (i != q3) {
      LAS char* p = S + 32768 + ((dh * 4 + i) * 3 + ((q3 - i - 1) & 3)) * 2048 + lane * 16;
      *(LAS f4*)p = f4{a[r0], a[r0 + 1], a[r0 + 2], a[r0 + 3]};
      *(LAS f4*)(p + 1024) = f4{a[r0 + 4], a[r0 + 5], a[r0 + 6], a[r0 + 7]};
    } else {
#pragma unroll
      for (int e = 0; e < 8; ++e) o8[e] = a[r0 + e];
    }
  }
  VMW(0);
  BAR();
  STAMP(12);
#pragma unroll
  for (int s = 0; s < 3; ++s)
#pragma unroll
    for (int u = 0; u < 2; ++u) {
      const f4 v = *(LAS f4*)(S + 32768 + ((dh * 4 + q3) * 3 + s) * 2048 + u * 1024 + lane * 16);
#pragma unroll
      for (int e = 0; e < 4; ++e) o8[4 * u + e] += v[e];
    }
#pragma unroll
  for (int u = 0; u < 2; ++u) {
    const v2u yv = trd(S + YI_OFF + (dl + 8 * u + 4 * hh + trq) * 128 + (ll + 16 * trb + 4 * trp) * 2);
    v2u o;
#pragma unroll
    for (int e = 0; e < 2; ++e) {
      const unsigned m = pk(o8[4 * u + 2 * e], o8[4 * u + 2 * e + 1]);
      const unsigned mb = pk(bf_lo(m) + bf_lo(bo[u][e]), bf_hi(m) + bf_hi(bo[u][e]));
      const unsigned xy = pk(bf_lo(xo[u][e]) + bf_lo(yv[e]), bf_hi(xo[u][e]) + bf_hi(yv[e]));
      o[e] = pk(bf_lo(xy) + bf_lo(mb), bf_hi(xy) + bf_hi(mb));
    }
    *(GAS v2u*)((GAS char*)out + (size_t)(64 * lt + ll + r31) * 1024 + (64 * dt + dl + 8 * u + 4 * hh) * 2) = o;
  }
  asm volatile(".Lfk_end:" ::: "memory");
}

extern "C" void kernel_launch(void* const* d_in, const int* in_sizes, int n_in, void* d_out, int out_size, void* d_ws, size_t ws_size,
                              hipStream_t stream) {
  (void)hipFuncSetAttribute((const void*)fused_k, hipFuncAttributeMaxDynamicSharedMemorySize, LDS_BYTES);
  fused_k<<<256, 512, LDS_BYTES, stream>>>((const u16*)d_in[0], (const u16*)d_in[1], (const u16*)d_in[2], (const u16*)d_in[3], (const u16*)d_in[4],
                                           (const u16*)d_in[5], (const u16*)d_in[6], (const u16*)d_in[7], (const u16*)d_in[8], (const u16*)d_in[10],
                                           (const u16*)d_in[11], (const u16*)d_in[12], (const u16*)d_in[13], (u16*)d_out, (char*)d_ws);
}
```

```cpp
#define STAMPS 1
#define PROBE 0
#define PROBEW 0
#include <hip/hip_runtime.h>
#include <stdint.h>

#define ABL_LAST "7"
#define DI __device__ __forceinline__
#define LAS __attribute__((address_space(3)))
#define GAS __attribute__((address_space(1)))
#define CAS __attribute__((address_space(4)))
typedef unsigned short u16;
typedef unsigned long long u64;
typedef unsigned v4u __attribute__((ext_vector_type(4)));
typedef unsigned v2u __attribute__((ext_vector_type(2)));
typedef float f2 __attribute__((ext_vector_type(2)));
typedef float f4 __attribute__((ext_vector_type(4)));
typedef float f16v __attribute__((ext_vector_type(16)));
typedef __bf16 bf8 __attribute__((ext_vector_type(8)));
typedef __bf16 bf2 __attribute__((ext_vector_type(2)));
typedef short s4 __attribute__((ext_vector_type(4)));
typedef __amdgpu_buffer_rsrc_t rsrc_t;

constexpr int SL = 2048, DM = 512, NM = 128, FF = 1792;
constexpr int WS_E = 0, WS_F0 = 4096, WS_FH = 32768, WS_FY = 98304, WS_ST = 163840, WS_CS = 180224, WS_XT = 262144;
constexpr int WS_W2 = WS_XT + (2 << 20), WS_Y = WS_W2 + (2 << 20), WS_H = WS_Y + (2 << 20), WS_END = WS_H + SL * FF * 2;
constexpr int SA = 98304, TROW = 528, TAB = 59 * TROW;
constexpr int LN_OFF = SA + 40 * TROW, LN_ROW = 1088, W2S_OFF = SA + TAB + 40 * TROW;
constexpr int KC = 4160, KCH = 4 * KC, KK_OFF = 0, XI_OFF = 2 * KCH, XI = 4608, CX_OFF = XI_OFF + 2 * XI, YI_OFF = 131072;
constexpr int AP_OFF = 160640, BP_OFF = AP_OFF + 1024, CSP_OFF = BP_OFF + 1024, CNT_OFF = CSP_OFF + 1024, STP_OFF = CNT_OFF + 32, LDS_BYTES = 163840;
static_assert(SA + 2 * TAB <= AP_OFF && CX_OFF + 16384 <= 65536 && STP_OFF + 32 <= LDS_BYTES, "lds map");
constexpr int SC1 = 16;
constexpr int SPIN = 1 << 16;

DI float bf_lo(unsigned u) { return __builtin_bit_cast(float, u << 16); }
DI float bf_hi(unsigned u) { return __builtin_bit_cast(float, u & 0xffff0000u); }
DI unsigned pk(float a, float b) { f2 v = {a, b}; return __builtin_bit_cast(unsigned, __builtin_convertvector(v, bf2)); }
DI float rbf(float a) { return bf_lo(pk(a, a)); }
DI f16v mfma32(v4u a, v4u b, f16v c) { return __builtin_amdgcn_mfma_f32_32x32x16_bf16(__builtin_bit_cast(bf8, a), __builtin_bit_cast(bf8, b), c, 0, 0, 0); }
DI f4 mfma16(v4u a, v4u b, f4 c) { return __builtin_amdgcn_mfma_f32_16x16x32_bf16(__builtin_bit_cast(bf8, a), __builtin_bit_cast(bf8, b), c, 0, 0, 0); }
DI v2u trd(LAS char* p) { return __builtin_bit_cast(v2u, __builtin_amdgcn_ds_read_tr16_b64_v4i16((LAS s4*)p)); }
DI v4u cat(v2u a, v2u b) { return v4u{a.x, a.y, b.x, b.y}; }
#define LD128(p) (*(LAS v4u*)(p))
#define LD64(p) (*(LAS v2u*)(p))
#define LD32(p) (*(LAS unsigned*)(p))
#define LD16(p) (*(LAS u16*)(p))
#define SCHED() __builtin_amdgcn_sched_barrier(0)
#define BAR() do { SCHED(); asm volatile("" ::: "memory"); __builtin_amdgcn_s_waitcnt(0xC07F); asm volatile("s_barrier" ::: "memory"); SCHED(); } while (0)
#define LP(a) ((LAS char*)(size_t)(unsigned)(a))
#define VMW(n) asm volatile("s_waitcnt vmcnt(" #n ")" ::: "memory")
DI v4u mk_srd(const void* p, unsigned bytes) { u64 a = (u64)p; return v4u{(unsigned)a, (unsigned)(a >> 32) & 0xffffu, bytes, 0x00020000u}; }
DI rsrc_t mk_rsrc(const void* p, int bytes) { return __builtin_amdgcn_make_buffer_rsrc((void*)p, 0, bytes, 0x00020000); }
DI void dma(v4u srd, int voff, int soff, unsigned lds) {
  asm volatile("s_mov_b32 m0, %3\n\ts_nop 0\n\tbuffer_load_dwordx4 %0, %1, %2 offen lds" :: "v"(voff), "s"(srd), "s"(soff), "s"(lds) : "memory");
}
DI void dma_sc1(v4u srd, int voff, int soff, unsigned lds) {
  asm volatile("s_mov_b32 m0, %3\n\ts_nop 0\n\tbuffer_load_dwordx4 %0, %1, %2 offen sc1 lds" :: "v"(voff), "s"(srd), "s"(soff), "s"(lds) : "memory");
}
DI void dma4_sc1(v4u srd, int voff, int soff, unsigned lds) {
  asm volatile("s_mov_b32 m0, %3\n\ts_nop 0\n\tbuffer_load_dwordx4 %0, %1, %2 offen sc1 lds\n\tbuffer_load_dwordx4 %0, %1, %2 offen offset:1024 sc1 lds\n\t"
               "buffer_load_dwordx4 %0, %1, %2 offen offset:2048 sc1 lds\n\tbuffer_load_dwordx4 %0, %1, %2 offen offset:3072 sc1 lds"
               :: "v"(voff), "s"(srd), "s"(soff), "s"(lds) : "memory");
}
template <int C> DI float dpp_add(float v) { return v + __builtin_bit_cast(float, __builtin_amdgcn_update_dpp(0, __builtin_bit_cast(int, v), C, 0xf, 0xf, true)); }
DI float rl(float v, int l) { return __builtin_bit_cast(float, __builtin_amdgcn_readlane(__builtin_bit_cast(int, v), l)); }
DI float wave_sum(float v) {
  v = dpp_add<0xB1>(v); v = dpp_add<0x4E>(v); v = dpp_add<0x141>(v); v = dpp_add<0x140>(v);
  return (rl(v, 0) + rl(v, 16)) + (rl(v, 32) + rl(v, 48));
}
DI f2 cmul(f2 a, f2 b) { return f2{a.x * b.x - a.y * b.y, a.x * b.y + a.y * b.x}; }
DI f2 cexp_(float lr2, float lrev, float e) {
  float m = __builtin_amdgcn_exp2f(lr2 * e), a = __builtin_amdgcn_fractf(lrev * e);
  return f2{m * __builtin_amdgcn_cosf(a), m * __builtin_amdgcn_sinf(a)};
}
DI u64 ld64(char* ws, int off) { return __hip_atomic_load((GAS u64*)(ws + off), __ATOMIC_RELAXED, __HIP_MEMORY_SCOPE_AGENT); }
#define ORDER() __builtin_amdgcn_fence(__ATOMIC_ACQUIRE, "wavefront")
DI void st64(rsrc_t r, int off, unsigned lo, unsigned hi) { __builtin_amdgcn_raw_buffer_store_b64(v2u{lo, hi}, r, off, 0, SC1); }

DI u64 rt() { u64 t; asm volatile("s_memrealtime %0\n\ts_waitcnt lgkmcnt(0)" : "=s"(t) :: "memory"); return t; }
DI u64 ct() { u64 t; asm volatile("s_memtime %0\n\ts_waitcnt lgkmcnt(0)" : "=s"(t) :: "memory"); return t; }
#define STAMP(k) do { SCHED(); stv[k] = rt(); SCHED(); } while (0)
#define DET(v)
#define DETACC(a, b)

#define STB(k) do {} while (0)
#define P1UNROLL _Pragma("unroll 1")
#define LD64V(p) (*(volatile LAS v2u*)(p))
#define LD32V(p) (*(volatile LAS unsigned*)(p))
struct cz { float r, i, n; };
DI cz mkz(f2 z) { return cz{z.x, z.y, -z.y}; }
DI cz conj(cz z) { return cz{z.r, z.n, z.i}; }
DI f2 cm(f2 a, cz z) {
  f2 o;
  asm("v_mul_f32 %0, %2, %4\n\tv_mul_f32 %1, %2, %5\n\tv_fmac_f32 %0, %3, %6\n\tv_fmac_f32 %1, %3, %4" : "=&v"(o.x), "=&v"(o.y) : "v"(a.x), "v"(a.y), "v"(z.r), "v"(z.i), "v"(z.n));
  return o;
}
DI void step2(f2& v, cz zv, f2& u, cz zu, unsigned& pv, unsigned& pu) {
  f2 nv, nu;
  asm("v_mul_f32 %0, %6, %10\n\tv_mul_f32 %2, %8, %13\n\tv_mul_f32 %1, %6, %11\n\tv_mul_f32 %3, %8, %14\n\t"
      "v_cvt_pk_bf16_f32 %4, %6, %7\n\tv_fmac_f32 %0, %7, %12\n\tv_fmac_f32 %2, %9, %15\n\tv_cvt_pk_bf16_f32 %5, %8, %9\n\t"
      "v_fmac_f32 %1, %7, %10\n\tv_fmac_f32 %3, %9, %13"
      : "=&v"(nv.x), "=&v"(nv.y), "=&v"(nu.x), "=&v"(nu.y), "=&v"(pv), "=&v"(pu)
      : "v"(v.x), "v"(v.y), "v"(u.x), "v"(u.y), "v"(zv.r), "v"(zv.i), "v"(zv.n), "v"(zu.r), "v"(zu.i), "v"(zu.n));
  v = nv; u = nu;
}
DI bool tag_ok(u64 f, unsigned lo, unsigned hi) { return __all((unsigned)f == lo && (unsigned)(f >> 32) == hi); }

__global__ __launch_bounds__(512, 2) void fused_k(const u16* __restrict__ x, const u16* __restrict__ Are, const u16* __restrict__ Aim,
    const u16* __restrict__ Bre, const u16* __restrict__ Bim, const u16* __restrict__ Cre, const u16* __restrict__ Cim,
    const u16* __restrict__ logdt, const u16* __restrict__ Dskip, const u16* __restrict__ w1, const u16* __restrict__ b1,
    const u16* __restrict__ w2, const u16* __restrict__ b2, u16* __restrict__ out, char* __restrict__ ws) {
  extern __shared__ __attribute__((aligned(16))) char smem[];
  LAS char* const S = (LAS char*)smem;
  u64 stv[16];
#pragma unroll
  for (int i = 0; i < 16; ++i) stv[i] = 0;
  STAMP(0);
  const u64 c0_ = ct();
  const int tid = threadIdx.x, lane = tid & 63, wave = __builtin_amdgcn_readfirstlane(tid >> 6);
  const int b = blockIdx.x, xcd = b & 7, j = b >> 3, g8 = xcd * 32 + j;
  if (wave >= 4) __builtin_amdgcn_s_setprio(3);
  asm volatile("" :: "s"(x), "s"(Are), "s"(Aim), "s"(Bre), "s"(Bim), "s"(Cre), "s"(Cim), "s"(logdt), "s"(Dskip), "s"(w1), "s"(b1), "s"(w2), "s"(b2), "s"(out), "s"(ws));
  v4u xr;
  asm volatile("global_load_dwordx4 %0, %1, %2" : "=v"(xr) : "v"(g8 * 8192 + tid * 16), "s"(x) : "memory");
  const bool mainb = j < 28;
  const int pl = j >= 14 ? 1 : 0, pp = xcd * 2 + pl, fs = j - 14 * pl;
  const int lt = xcd * 4 + (j >> 3), dt = j & 7;
  const int ch0 = xcd * 64 + j * 2;
  const rsrc_t rw = mk_rsrc(ws, WS_END);
  const v4u sw_ = mk_srd(ws, WS_END);
  const int r31 = lane & 31, hh = lane >> 5, trq = (lane & 15) >> 2, trp = lane & 3, trb = (lane >> 4) & 1;
  const int q3 = wave & 3, dh = wave >> 2;
  v4u w2f[28];
  const int w2off = WS_W2 + ((2 * dt + dh) * 112 + 28 * q3) * 1024 + lane * 16;
  unsigned tlo, thi;
#define ROW_WAIT(n) float v_[8]; asm volatile("s_waitcnt vmcnt(" #n ")\n\tv_lshlrev_b32 %0, 16, %8\n\tv_and_b32 %1, 0xffff0000, %8\n\tv_lshlrev_b32 %2, 16, %9\n\tv_and_b32 %3, 0xffff0000, %9\n\t" \
    "v_lshlrev_b32 %4, 16, %10\n\tv_and_b32 %5, 0xffff0000, %10\n\tv_lshlrev_b32 %6, 16, %11\n\tv_and_b32 %7, 0xffff0000, %11" \
    : "=&v"(v_[0]), "=&v"(v_[1]), "=&v"(v_[2]), "=&v"(v_[3]), "=&v"(v_[4]), "=&v"(v_[5]), "=&v"(v_[6]), "=&v"(v_[7]) : "v"(xr[0]), "v"(xr[1]), "v"(xr[2]), "v"(xr[3]) : "memory")
  auto layernorm = [&](float (&v)[8]) {
    float s;
    s = ((v[0] + v[1]) + (v[2] + v[3])) + ((v[4] + v[5]) + (v[6] + v[7]));
    const float mf = wave_sum(s) * (1.f / DM);
    float q0 = 0.f, q1 = 0.f;
#pragma unroll
    for (int e = 0; e < 4; ++e) { float d = v[2 * e] - mf, f = v[2 * e + 1] - mf; q0 += d * d; q1 += f * f; }
    const float var = rbf(wave_sum(q0 + q1) * (1.f / DM));
    const float m = rbf(mf), sd = rbf(__builtin_amdgcn_sqrtf(rbf(var + 1e-5f))), rs = __builtin_amdgcn_rcpf(sd);
    v4u o;
#pragma unroll
    for (int e = 0; e < 4; ++e) { unsigned d = pk(v[2 * e] - m, v[2 * e + 1] - m); o[e] = pk(bf_lo(d) * rs, bf_hi(d) * rs); }
    LD128(S + LN_OFF + wave * LN_ROW + lane * 16) = o;
    if (lane == 0) LD32(S + STP_OFF + wave * 4) = pk(-m, sd);
  };
  const int hs = WS_H + (2 * lt * 112 + 28 * q3) * 1024, hl = q3 * 8192;

  if (wave < 4) {
    const int gw = wave, wf = gw & 1, wl = gw >> 1;
    if (mainb) {
      const v4u sx = mk_srd(x, SL * DM * 2), s1 = mk_srd(w1, DM * FF * 2);
      int vx[4], v1[4];
#pragma unroll
      for (int i = 0; i < 4; ++i) {
        vx[i] = (lane >> 3) * 1024 + (((lane & 7) ^ ((4 * (i & 1) + (lane >> 4)) & 7)) << 4) + i * 7168;
        v1[i] = (lane >> 4) * 3584 + (((lane & 15) ^ ((lane >> 4) << 2) ^ i) << 4) + i * 13312;
      }
      const int rot = (fs + 4 * pl + xcd) & 7;
      const int sx0 = (pp * 128 + 32 * gw) * 1024, s10 = 16 * gw * 3584 + fs * 256;
      auto issue8 = [&](int s) {
        const int ks = (s + rot) & 7;
        asm volatile("s_mov_b32 m0, %12\n\ts_nop 0\n\tbuffer_load_dwordx4 %0, %8, %10 offen lds\n\tbuffer_load_dwordx4 %1, %8, %10 offen offset:1024 lds\n\t"
                     "buffer_load_dwordx4 %2, %8, %10 offen offset:2048 lds\n\tbuffer_load_dwordx4 %3, %8, %10 offen offset:3072 lds\n\t"
                     "s_add_u32 m0, m0, 0x4000\n\ts_nop 0\n\tbuffer_load_dwordx4 %4, %9, %11 offen lds\n\tbuffer_load_dwordx4 %5, %9, %11 offen offset:1024 lds\n\t"
                     "buffer_load_dwordx4 %6, %9, %11 offen offset:2048 lds\n\tbuffer_load_dwordx4 %7, %9, %11 offen offset:3072 lds"
                     :: "v"(vx[0]), "v"(vx[1]), "v"(vx[2]), "v"(vx[3]), "v"(v1[0]), "v"(v1[1]), "v"(v1[2]), "v"(v1[3]), "s"(sx), "s"(s1),
                        "s"(sx0 + ks * 128), "s"(s10 + ks * 229376), "s"(s * 32768 + gw * 4096) : "memory", "scc");
      };
      issue8(0); issue8(1);
      SCHED();
      STAMP(13);
      f16v acc[2][2];
#pragma unroll
      for (int a = 0; a < 2; ++a)
#pragma unroll
        for (int c = 0; c < 2; ++c) {
#pragma unroll
          for (int i = 0; i < 16; ++i) acc[a][c][i] = 0.f;
          asm volatile("" : "+v"(acc[a][c]));
        }
      int xa[4], wa[4];
#pragma unroll
      for (int i = 0; i < 4; ++i) {
        const int ft = i >> 1, t = i & 1;
        xa[i] = (64 * wl + r31) * 128 + (((2 * i + hh) ^ ((r31 >> 1) & 7)) << 4);
        wa[i] = (8 * hh + 4 * t + trq) * 256 + (((4 * (2 * wf + ft) + 2 * trb + (trp >> 1)) ^ ((trq << 2) | ((2 * hh + t) & 3))) << 4) + 8 * (trp & 1);
        asm volatile("" : "+v"(xa[i]), "+v"(wa[i]));
      }
      ROW_WAIT(16);
      layernorm(v_);
      SCHED();
      issue8(2);
      v2u FA[2][4][4]; v4u FB[2][4][2];
      auto rd = [&](int set, int n, bool first = false) {
        const int ks = n / 6, i = n % 6;
        if (i == 2 || i == 3) FB[set][ks][i - 2] = LD128(LP(xa[ks]) + (i - 2) * 4096);
        else { const int k = i < 2 ? i : i - 2; FA[set][ks][k] = trd(LP(wa[k]) + 16384 + ks * 4096); }
      };
      VMW(16);
      STAMP(7);
      STB(1);
      BAR();
      STAMP(1);
#pragma unroll
      for (int n = 0; n < 24; ++n) rd(0, n, true);
      int slot = 0;
      auto stage = [&](int set, int s) {
        const int dsl = slot == 65536 ? -65536 : 32768;
        const int ks = (s + 3 + rot) & 7, so_x = sx0 + ks * 128, so_w = s10 + ks * 229376, m0v = slot + gw * 4096;
        STB(2 + s);
        DET(d0_);
        asm volatile("s_cmp_lt_i32 %0, 6\n\ts_cbranch_scc1 1f\n\ts_waitcnt vmcnt(0)\n1:\n\ts_waitcnt vmcnt(8)" :: "s"(s) : "memory", "scc");
        DET(d1_);
        BAR();
        DET(d2_);
        DETACC(5, 6);
#pragma unroll
        for (int n = 0; n < 16; ++n) {
          const int k = n >> 2, a = (n >> 1) & 1, c = n & 1;
          acc[a][c] = mfma32(cat(FA[set][k][2 * a], FA[set][k][2 * a + 1]), FB[set][k][c], acc[a][c]);
          SCHED();
          if (n == 0) {
#pragma unroll
            for (int i = 0; i < 4; ++i) wa[i] += dsl;
            xa[0] += dsl;
          }
          if (n >= 1 && n <= 12) { rd(set ^ 1, 2 * n - 2); rd(set ^ 1, 2 * n - 1); }
          if (n == 1 || n == 4 || n == 7) xa[n / 3 + 1] += dsl;
          if (n >= 8) {
            const int i = n & 3;
            if (n == 8) asm volatile("s_cmp_gt_i32 %4, " ABL_LAST "\n\ts_cbranch_scc1 1f\n\ts_mov_b32 m0, %3\n\ts_nop 0\n\tbuffer_load_dwordx4 %0, %1, %2 offen lds\n1:" :: "v"(vx[0]), "s"(sx), "s"(so_x), "s"(m0v), "s"(s + 3) : "memory", "scc");
            else if (n == 12) asm volatile("s_cmp_gt_i32 %4, " ABL_LAST "\n\ts_cbranch_scc1 1f\n\ts_mov_b32 m0, %3\n\ts_nop 0\n\tbuffer_load_dwordx4 %0, %1, %2 offen lds\n1:" :: "v"(v1[0]), "s"(s1), "s"(so_w), "s"(m0v + 16384), "s"(s + 3) : "memory", "scc");
            else asm volatile("s_cmp_gt_i32 %4, " ABL_LAST "\n\ts_cbranch_scc1 1f\n\tbuffer_load_dwordx4 %0, %1, %2 offen offset:%3 lds\n1:" :: "v"(n < 12 ? vx[i] : v1[i]), "s"(n < 12 ? sx : s1), "s"(n < 12 ? so_x : so_w), "i"(i * 1024), "s"(s + 3) : "memory", "scc");
          }
          SCHED();
        }
        slot += dsl;
      };
P1UNROLL
      for (int s = 0; s < 8; s += 2) { stage(0, s); stage(1, s + 1); }
      STB(10);
      STAMP(2);
      v4u Ap[2], Bp[2]; float sd[2];
#pragma unroll
      for (int i = 0; i < 2; ++i) {
        v2u a = LD64(S + AP_OFF + (64 * wf + 32 * i + r31) * 8), bb = LD64(S + BP_OFF + (64 * wl + 32 * i + r31) * 8);
        sd[i] = bf_hi(bb.y);
        Ap[i] = v4u{hh ? 0u : a.x, hh ? 0u : a.y, 0u, 0u};
        Bp[i] = v4u{hh ? 0u : bb.x, hh ? 0u : bb.y, 0u, 0u};
      }
#pragma unroll
      for (int c = 0; c < 2; ++c)
#pragma unroll
        for (int a = 0; a < 2; ++a) acc[a][c] = mfma32(Ap[a], Bp[c], acc[a][c]);
      STAMP(3);
#pragma unroll
      for (int c = 0; c < 2; ++c) {
        const float s = sd[c], rs = __builtin_amdgcn_rcpf(s);
        const float c0 = -2.3022081985f * rs, c1 = c0 * 0.044715f * rs * rs;
        const f2 c0v = {c0, c0}, c1v = {c1, c1}, sv = {s, s};
#pragma unroll
        for (int a = 0; a < 2; ++a)
#pragma unroll
          for (int u = 0; u < 2; ++u) {
            v4u o;
#pragma unroll
            for (int e = 0; e < 4; ++e) {
              f2 v = {acc[a][c][8 * u + 2 * e], acc[a][c][8 * u + 2 * e + 1]};
              f2 z = v * __builtin_elementwise_fma(v * v, c1v, c0v);
              f2 ex = {__builtin_amdgcn_exp2f(z.x), __builtin_amdgcn_exp2f(z.y)};
              f2 d = __builtin_elementwise_fma(ex, sv, sv);
              f2 r = {__builtin_amdgcn_rcpf(d.x), __builtin_amdgcn_rcpf(d.y)};
              f2 g = v * r;
              o[e] = pk(g.x, g.y);
            }
            __builtin_amdgcn_raw_buffer_store_b128(o, rw, lane * 16 + (WS_H + ((4 * pp + 2 * wl + c) * 112 + 8 * fs + 4 * wf + 2 * a + u) * 1024), 0, SC1);
          }
      }
    } else {
      const int sp = min(xcd * 4 + (j - 28), 27);
      float cs[8];
#pragma unroll
      for (int i = 0; i < 8; ++i) cs[i] = 0.f;
      const GAS char* p = (const GAS char*)w1 + (size_t)(gw * 128 + (lane >> 3)) * 3584 + sp * 128 + (lane & 7) * 16;
      v4u v[16];
#pragma unroll
      for (int i = 0; i < 16; ++i) v[i] = *(const GAS v4u*)(p + i * 8 * 3584);
      SCHED();
      ROW_WAIT(16);
      layernorm(v_);
      BAR();
      STAMP(1);
#pragma unroll
      for (int i = 0; i < 16; ++i)
#pragma unroll
        for (int e = 0; e < 4; ++e) { cs[2 * e] += bf_lo(v[i][e]); cs[2 * e + 1] += bf_hi(v[i][e]); }
#pragma unroll
      for (int i = 0; i < 8; ++i) { cs[i] += __shfl_xor(cs[i], 8); cs[i] += __shfl_xor(cs[i], 16); cs[i] += __shfl_xor(cs[i], 32); }
      if (lane < 8) {
        *(LAS f4*)(S + CSP_OFF + gw * 256 + lane * 32) = f4{cs[0], cs[1], cs[2], cs[3]};
        *(LAS f4*)(S + CSP_OFF + gw * 256 + lane * 32 + 16) = f4{cs[4], cs[5], cs[6], cs[7]};
      }
      BAR();
      if (xcd * 4 + (j - 28) < 28 && gw == 0) {
        const unsigned tl = LD32(S + CNT_OFF + 8);
        LAS float* c = (LAS float*)(S + CSP_OFF);
        float t = (c[lane] + c[64 + lane]) + (c[128 + lane] + c[192 + lane]);
        st64(rw, WS_CS + (sp * 64 + lane) * 8, __builtin_bit_cast(unsigned, t), tl);
      }
      STAMP(2);
#pragma unroll 1
      for (int s = 0; s < 7; ++s) BAR();
    }
    SCHED();
    if (lane == 0) LD32V(S + CNT_OFF + 16 + gw * 4) = 1u;
    { v2u t = LD64(S + CNT_OFF + 8); tlo = __builtin_amdgcn_readfirstlane(t.x); thi = __builtin_amdgcn_readfirstlane(t.y); }
    STAMP(4);
#pragma unroll 1
    for (int sp = 0; sp < SPIN && LD32V(S + CNT_OFF) < 4u; ++sp) __builtin_amdgcn_s_sleep(1);
    asm volatile("" ::: "memory");
    SCHED();
#pragma unroll
    for (int i = 0; i < 14; ++i) w2f[i] = __builtin_amdgcn_raw_buffer_load_b128(rw, w2off + i * 1024, 0, SC1);
    SCHED();
    VMW(14);
    if (mainb && lane == 0) st64(rw, WS_FH + (((pp * 2 + wl) * 14 + fs) * 2 + wf) * 64, tlo, thi);
    STAMP(5);
    SCHED();
#pragma unroll
    for (int i = 14; i < 28; ++i) w2f[i] = __builtin_amdgcn_raw_buffer_load_b128(rw, w2off + i * 1024, 0, SC1);
    STAMP(6);
    {
      const int fo = WS_FH + (lt * 28 + 7 * q3 + min(lane, 6)) * 64;
#pragma unroll 1
      for (int it = 0; it < SPIN; ++it) if (tag_ok(ld64(ws, fo), tlo, thi)) break;
    }
    STAMP(9);
#pragma unroll
    for (int t = 0; t < 2; ++t)
#pragma unroll
      for (int c = 0; c < 2; ++c) dma4_sc1(sw_, lane * 16, hs + c * 114688 + t * 4096, (t + 2) * 32768 + hl + c * 4096);
    BAR();
#pragma unroll
    for (int c = 0; c < 2; ++c) dma4_sc1(sw_, lane * 16, hs + c * 114688 + 2 * 4096, hl + c * 4096);
    VMW(16);
  } else {
    const int sw = wave - 4, cc = sw >> 1, sh = sw & 1, ch = ch0 + cc;
    const v2u ev = __builtin_amdgcn_raw_buffer_load_b64(rw, WS_E, 0, SC1);
    const int pi = ch * NM + 64 * sh + lane;
    const u16 p_ar = Are[pi], p_ai = Aim[pi], p_br = Bre[pi], p_bi = Bim[pi], p_cr = Cre[pi], p_ci = Cim[pi];
    SCHED();
    const int pid = (xcd * 28 + min(j, 27)) * 4 + sw;
    {
      const v4u s2 = mk_srd(w2, FF * DM * 2);
      const int so = (pid >> 3) * 16 * 1024 + (pid & 7) * 128, vo = (lane >> 3) * 1024 + (lane & 7) * 16;
      dma(s2, vo, so, W2S_OFF + sw * 2048);
      dma(s2, vo, so + 8192, W2S_OFF + sw * 2048 + 1024);
    }
    unsigned codew;
    {
      u64 pc; unsigned csz;
      asm volatile("s_getpc_b64 %0\n.Lfk_begin:\n\ts_mov_b32 %1, .Lfk_end-.Lfk_begin-4" : "=s"(pc), "=s"(csz));
      unsigned o = min((unsigned)(j * 8 + sw * 2 + (lane & 1)) * 128u, csz);
      asm volatile("global_load_dword %0, %1, %2" : "=v"(codew) : "v"(o), "s"(pc) : "memory");
    }
    __builtin_amdgcn_s_setprio(0);
    const unsigned ldt2 = *(const CAS unsigned*)(logdt + ch0), dsk2 = *(const CAS unsigned*)(Dskip + ch0);
    const float ldt = cc ? bf_hi(ldt2) : bf_lo(ldt2), dsk = cc ? bf_hi(dsk2) : bf_lo(dsk2);
    ROW_WAIT(10);
    STAMP(13);
    layernorm(v_);
    { u64 e = (((u64)ev.y << 32) | ev.x) + 1; tlo = __builtin_amdgcn_readfirstlane((unsigned)e); thi = __builtin_amdgcn_readfirstlane((unsigned)(e >> 32)); }
    if (tid == 256) { *(LAS v4u*)(S + CNT_OFF) = v4u{0u, 0u, tlo, thi}; *(LAS v4u*)(S + CNT_OFF + 16) = v4u{0u, 0u, 0u, 0u}; }
    STAMP(1);
    STB(1);
    BAR();
    VMW(0);
    asm volatile("" :: "v"(codew));
    if (tid >= 504) st64(rw, WS_ST + (g8 * 8 + (lane & 7)) * 8, LD32(S + STP_OFF + (lane & 7) * 4), tlo);
#pragma unroll
    for (int u = 0; u < 2; ++u) {
      LAS char* p = S + LN_OFF + trq * LN_ROW + (128 * sw + 64 * u + (lane & 48) + 4 * trp) * 2;
      v4u o = cat(trd(p), trd(p + 4 * LN_ROW));
      __builtin_amdgcn_raw_buffer_store_b128(o, rw, lane * 16 + (WS_XT + g8 * 8192 + (128 * sw + 64 * u) * 16), 0, SC1);
    }
    if (mainb) {
#pragma unroll
      for (int u = 0; u < 2; ++u) {
        LAS char* p = S + W2S_OFF + sw * 2048 + (4 * hh + trq) * 128 + (32 * u + 16 * trb + 4 * trp) * 2;
        v4u o = cat(trd(p), trd(p + 1024));
        __builtin_amdgcn_raw_buffer_store_b128(o, rw, lane * 16 + (WS_W2 + ((2 * (pid & 7) + u) * 112 + (pid >> 3)) * 1024), 0, SC1);
      }
    }
    STAMP(2);
    STB(2);
    BAR();
    const float dtv = rbf(__builtin_amdgcn_exp2f(ldt * 1.4426950408889634f));
    float lr2 = (dtv * bf_lo(p_ar)) * 1.4426950408889634f, lrev = (dtv * bf_lo(p_ai)) * 0.15915494309189535f;
    asm("" : "+v"(lr2));
    float dbr = dtv * bf_lo(p_br);
    asm("" : "+v"(dbr));
    const f2 cb = cm(f2{bf_lo(p_cr), -bf_lo(p_ci)}, conj(mkz(f2{dbr, dtv * bf_lo(p_bi)})));
    const cz z8 = mkz(cexp_(lr2, lrev, 8.f)), z8c = conj(z8), z1 = conj(mkz(cexp_(lr2, lrev, 1.f))), z256 = conj(mkz(cexp_(lr2, lrev, 256.f)));
    const cz z2 = mkz(cm(f2{z1.r, z1.i}, z1)), z4 = mkz(cm(f2{z2.r, z2.i}, z2));
    LAS char* const T = S + SA + cc * TAB + (64 * sh + lane) * 4;
    f2 V = {1.f, 0.f}, U = cb, W = cb;
    auto put = [&](int r, cz zu) {
 unsigned pv, pu; step2(V, z8, U, zu, pv, pu); LD32V(T + r * TROW) = pv; LD32V(T + (32 + r) * TROW) = pu; };
    STB(3);
    BAR();
#pragma unroll
    for (int i = 0; i < 6; ++i) put(i, z1);
    STB(4);
    BAR();
    put(6, z1); put(7, z1);
    W = cm(W, z256); U = W;
#pragma unroll
    for (int i = 0; i < 4; ++i) put(8 + i, z2);
    VMW(0);
    STB(5);
    BAR();
    if (tid == 256) st64(rw, WS_F0 + b * 64, tlo, thi);
    put(12, z2);
    W = cm(W, z256); U = W;
    put(13, z4); put(14, z4); put(15, z4);
    W = cm(W, z256); U = W;
    put(16, z4); put(17, z4);
    STB(6);
    BAR();
    const int goff = cc == 0 ? WS_ST + (pp * 128 + 64 * sh + lane) * 8 : WS_CS + (min(fs, 13) * 128 + 64 * sh + lane) * 8;
    const unsigned b1v = b1[min(fs, 13) * 128 + 64 * sh + lane];
    u64 gr = ld64(ws, goff);
    put(18, z4);
#pragma unroll
    for (int k = 0; k < 2; ++k) { W = cm(W, z256); U = W; put(19 + 2 * k, z8c); put(20 + 2 * k, z8c); }
    STB(7);
    BAR();
    const int pg = 64 * sw + lane;
    const int foff = WS_F0 + (((pg & 31) << 3) | (pg >> 5)) * 64;
    u64 f0 = ld64(ws, foff);
#pragma unroll
    for (int k = 2; k < 4; ++k) { W = cm(W, z256); U = W; put(19 + 2 * k, z8c); put(20 + 2 * k, z8c); }
    STB(8);
    BAR();
#pragma unroll
    for (int i = 27; i < 32; ++i) { LD32V(T + i * TROW) = pk(V.x, V.y); V = cm(V, z8); }
    if (mainb) {
#pragma unroll 1
      for (int sp = 0; sp < SPIN && !__all((unsigned)(gr >> 32) == tlo); ++sp) gr = ld64(ws, goff);
      const unsigned pv = (unsigned)gr;
      if (cc == 0) {
        const unsigned nm = pv & 0xffffu;
        LD64(S + BP_OFF + (64 * sh + lane) * 8) = v2u{nm | (nm << 16), pv};
      } else {
        const float c = __builtin_bit_cast(float, pv), c_hi = rbf(c), r1 = c - c_hi, c_mid = rbf(r1), r2 = r1 - c_mid;
        LD64(S + AP_OFF + (64 * sh + lane) * 8) = v2u{pk(c_hi, c_mid), (pk(r2, r2) & 0xffffu) | (b1v << 16)};
      }
    }
    STAMP(3);
    STB(9);
    BAR();
#pragma unroll 1
    for (int it = 0; it < SPIN && !tag_ok(f0, tlo, thi); ++it) f0 = ld64(ws, foff);
    ORDER();
    if (lane == 0) __hip_atomic_fetch_add((LAS unsigned*)(S + CNT_OFF + 4), 1u, __ATOMIC_RELAXED, __HIP_MEMORY_SCOPE_WORKGROUP);
    v4u xv[2];
#pragma unroll
    for (int u = 0; u < 2; ++u) xv[u] = __builtin_amdgcn_raw_buffer_load_b128(rw, (lane >> 1) * 8192 + (lane & 1) * 16, WS_XT + (64 * sw + 32 * u) * 8192 + ch0 * 16, SC1);
    auto ldw = [&](int i) { w2f[i] = __builtin_amdgcn_raw_buffer_load_b128(rw, w2off + (i & 3) * 1024, (i >> 2) * 4096, SC1); };
    f16v kk;
    {
      const int t = r31 & 7, bk = r31 >> 3;
      int ur;
      if (sh) ur = 19 + 2 * bk + (t >= 5 ? 1 : 0);
      else ur = bk == 0 ? t : (bk == 1 ? 8 + ((0x43222100u >> (4 * t)) & 15) : 7 + 3 * bk + ((0x22111000u >> (4 * t)) & 15));
      LAS char* pa = S + SA + cc * TAB + (32 + ur) * TROW + hh * 16;
      LAS char* pb = S + SA + cc * TAB + r31 * TROW + hh * 16;
#pragma unroll
      for (int i = 0; i < 16; ++i) kk[i] = 0.f;
      v4u A[16], B[16];
#pragma unroll
      for (int s = 0; s < 6; ++s) { A[s] = LD128(pa + s * 32); B[s] = LD128(pb + s * 32); }
#pragma unroll 1
      for (int sp = 0; sp < SPIN && LD32V(S + CNT_OFF + 4) < 4u; ++sp) __builtin_amdgcn_s_sleep(1);
      asm volatile("" ::: "memory");
      if (b == 0 && tid == 256) st64(rw, WS_E, tlo, thi);
      STAMP(4);
      SCHED();
#pragma unroll
      for (int s = 0; s < 16; ++s) {
        if (s + 6 < 16) { A[s + 6] = LD128(pa + (s + 6) * 32); B[s + 6] = LD128(pb + (s + 6) * 32); }
        kk = mfma32(A[s], B[s], kk);
        if (s < 12) ldw(s);
        SCHED();
      }
    }
    STAMP(5);
    {
      LAS char* kb = S + KK_OFF + cc * KCH + 8;
#pragma unroll
      for (int g = 0; g < 4; ++g) {
        float k0 = kk[4 * g], k1 = kk[4 * g + 1], k2 = kk[4 * g + 2], k3 = kk[4 * g + 3];
        if (sh && hh && (r31 & 1)) k0 = k1;
        const unsigned e01 = pk(k3, k2), e23 = pk(k1, k0), e12 = __builtin_amdgcn_alignbit(e23, e01, 16);
        LAS char* p = kb + (2044 - (256 * (4 * sh + 3) + 8 * r31 + 4 * hh)) * 2 + (3 - g) * 512;
        LD64(p) = v2u{e01, e23};
        LD16(p + KC - 2) = (u16)e01; LD32V(p + KC) = e12; LD16(p + KC + 4) = (u16)(e23 >> 16);
        LD32V(p + 2 * KC - 4) = e01; LD32V(p + 2 * KC) = e23;
        LD16(p + 3 * KC - 6) = (u16)e01; LD32V(p + 3 * KC - 4) = e12; LD16(p + 3 * KC) = (u16)(e23 >> 16);
        ldw(12 + 2 * g); ldw(13 + 2 * g);
        SCHED();
      }
      if (sh == 0) {
        const int s = lane >> 4, i = lane & 15;
        LD16(kb + s * KC + (2048 - s + i) * 2) = 0;
      } else if (lane < 30) {
        LD128(S + XI_OFF + cc * XI + lane * 16) = v4u{0u, 0u, 0u, 0u};
      }
#pragma unroll
      for (int u = 0; u < 2; ++u) LD128(S + XI_OFF + (lane & 1) * XI + 480 + (64 * sw + 32 * u + (lane >> 1)) * 16) = xv[u];
      asm volatile("s_waitcnt lgkmcnt(0)" ::: "memory");
      if (lane == 0) __hip_atomic_fetch_add((LAS unsigned*)(S + CNT_OFF), 1u, __ATOMIC_RELAXED, __HIP_MEMORY_SCOPE_WORKGROUP);
#pragma unroll
      for (int i = 20; i < 28; ++i) ldw(i);
      SCHED();
    }
#pragma unroll 1
    for (int sp = 0; sp < SPIN && LD32V(S + CNT_OFF) < 4u; ++sp) __builtin_amdgcn_s_sleep(1);
    asm volatile("" ::: "memory");
    STAMP(6);
    f4 ya[8];
#pragma unroll
    for (int i = 0; i < 8; ++i) ya[i] = f4{0.f, 0.f, 0.f, 0.f};
    {
      const int m = lane & 15, g = lane >> 4;
      LAS char* pa = S + KK_OFF + cc * KCH + (3 - (m & 3)) * KC + 8 + (2044 - 16 * (16 * 7 + 2 * (4 * sh + 3) + 1) - 4 * (m >> 2) + 8 * g) * 2;
      LAS char* pb = S + XI_OFF + cc * XI + (30 + 2 * m - 4 * (4 * sh + 3) - 2 + g) * 16;
      v4u A[4][8], B[4][8];
      auto lda = [&](int q, int p) { LAS char* a = pa + 512 * (7 - p) + 64 * (3 - q); A[q][p] = cat(LD64V(a), LD64V(a + 8)); };
      auto ldb = [&](int q, int e) { B[q][e] = *(volatile LAS v4u*)(pb + 512 * e + 64 * (3 - q)); };
      lda(0, 0);
#pragma unroll
      for (int e = 7; e >= 0; --e) ldb(0, e);
      lda(0, 1); lda(0, 2);
#pragma unroll 1
      for (int sp = 0; sp < SPIN && LD32V(S + CNT_OFF + 16 + sw * 4) == 0u; ++sp) __builtin_amdgcn_s_sleep(1);
      STAMP(9);
      SCHED();
#pragma unroll
      for (int q = 0; q < 4; ++q) {
        int n = 0;
#pragma unroll
        for (int p = 0; p < 8; ++p)
#pragma unroll
          for (int e = 7 - p; e >= 0; --e) {
            ya[p + e] = mfma16(A[q][p], B[q][e], ya[p + e]);
            if (n == 2) lda(q, 3);
            if (n == 8) lda(q, 4);
            if (n == 12) lda(q, 5);
            if (n == 16) lda(q, 6);
            if (n == 18) lda(q, 7);
            if (q < 3) {
              if (n == 20) lda(q + 1, 0);
              if (n == 28) lda(q + 1, 1);
              if (n == 34) lda(q + 1, 2);
              if (n == 4) ldb(q + 1, 7);
              if (n == 10) ldb(q + 1, 6);
              if (n == 17) ldb(q + 1, 5);
              if (n == 22) ldb(q + 1, 4);
              if (n == 24) ldb(q + 1, 3);
              if (n == 26) ldb(q + 1, 2);
              if (n == 30) ldb(q + 1, 1);
              if (n == 31) ldb(q + 1, 0);
            }
            SCHED();
            ++n;
          }
      }
    }
    f4 own[4];
#pragma unroll
    for (int i = 0; i < 4; ++i) {
      *(LAS f4*)(S + CX_OFF + sw * 4096 + i * 1024 + lane * 16) = sh ? ya[i] : ya[4 + i];
      own[i] = sh ? ya[4 + i] : ya[i];
    }
    STAMP(7);
    BAR();
    __builtin_amdgcn_s_waitcnt(0x0F70);
#pragma unroll
    for (int i = 0; i < 4; ++i) {
      const f4 y = own[i] + *(LAS f4*)(S + CX_OFF + (sw ^ 1) * 4096 + i * 1024 + lane * 16);
      const int l = 256 * (4 * sh + i) + 16 * (lane & 15) + 4 * (lane >> 4);
      const v2u xq = LD64(S + XI_OFF + cc * XI + 480 + l * 2);
      const unsigned y01 = pk(y[0], y[1]), y23 = pk(y[2], y[3]);
      const unsigned d01 = pk(bf_lo(xq.x) * dsk, bf_hi(xq.x) * dsk), d23 = pk(bf_lo(xq.y) * dsk, bf_hi(xq.y) * dsk);
      st64(rw, WS_Y + ch * 4096 + l * 2, pk(bf_lo(y01) + bf_lo(d01), bf_hi(y01) + bf_hi(d01)), pk(bf_lo(y23) + bf_lo(d23), bf_hi(y23) + bf_hi(d23)));
    }
  }
  STAMP(8);
  BAR();
  STAMP(10);
  const int hso = hs + dh * 114688, hlo = hl + dh * 4096;
  LAS char* const hp = S + hl + lane * 16;
  LAS char* hp2 = hp + 65536;
  asm volatile("" : "+v"(hp2));
  v4u H[2][8];
  auto ldh = [&](int t, int i) { const int sl = (t + 2) & 3; H[t & 1][i] = LD128((sl & 2 ? hp2 : hp) + (sl & 1) * 32768 + i * 1024); };
#pragma unroll
  for (int i = 0; i < 4; ++i) { ldh(0, i); ldh(0, 4 + i); }
  dma4_sc1(sw_, lane * 16, hso + 3 * 4096, 32768 + hlo);
  f16v acc[2];
#pragma unroll
  for (int c = 0; c < 2; ++c)
#pragma unroll
    for (int i = 0; i < 16; ++i) acc[c][i] = 0.f;
  const int dl = 32 * dh + 16 * (q3 & 1), ll = 32 * (q3 >> 1);
  const int fyo = WS_FY + ((lt >> 4) * 512 + 64 * dt + 8 * wave + (lane & 7)) * 64;
  u64 fy = 0;
  v2u xo[2], bo[2];
#pragma unroll
  for (int t = 0; t < 7; ++t) {
    if (t < 6) {
      DET(d0_);
      if (t == 0) VMW(12); else if (t < 4) VMW(8); else if (t == 4) VMW(4); else VMW(0);
      DET(d1_);
      BAR();
      DET(d2_);
      DETACC(3, 4);
      if (t == 2 && wave >= 4 && lane == 0) st64(rw, WS_FY + (((wave & 1) * 512) + ch0 + ((wave >> 1) & 1)) * 64, tlo, thi);
    }
    if (t == 5) {
#pragma unroll 1
      for (int it = 0; it < SPIN && !tag_ok(fy, tlo, thi); ++it) fy = ld64(ws, fyo);
      dma_sc1(sw_, (lane >> 3) * 4096 + (lane & 7) * 16, WS_Y + (64 * dt + 8 * wave) * 4096 + lt * 128, YI_OFF + wave * 1024);
    }
    SCHED();
#pragma unroll
    for (int i = 0; i < 4; ++i) {
      acc[0] = mfma32(w2f[4 * t + i], H[t & 1][i], acc[0]);
      SCHED();
      if (t < 6 && i < 2) { ldh(t + 1, 2 * i); ldh(t + 1, 4 + 2 * i); }
      SCHED();
      acc[1] = mfma32(w2f[4 * t + i], H[t & 1][4 + i], acc[1]);
      SCHED();
      if (t < 6 && i < 2) { ldh(t + 1, 2 * i + 1); ldh(t + 1, 5 + 2 * i); }
      SCHED();
      if (i == 2 && t <= 2) dma4_sc1(sw_, lane * 16, hso + (t + 4) * 4096, ((t + 2) & 3) * 32768 + hlo);
      if (i == 2 && t == 4) {
        fy = ld64(ws, fyo);
#pragma unroll
        for (int u = 0; u < 2; ++u) {
          xo[u] = *(const GAS v2u*)((const GAS char*)x + (size_t)(64 * lt + ll + r31) * 1024 + (64 * dt + dl + 8 * u + 4 * hh) * 2);
          bo[u] = *(const GAS v2u*)((const GAS char*)b2 + (64 * dt + dl + 8 * u + 4 * hh) * 2);
        }
      }
      SCHED();
    }
  }
  STAMP(11);
  float o8[8];
#pragma unroll
  for (int i = 0; i < 4; ++i) {
    const f16v& a = acc[i >> 1];
    const int r0 = 8 * (i & 1);
    if (i != q3) {
      LAS char* p = S + 32768 + ((dh * 4 + i) * 3 + ((q3 - i - 1) & 3)) * 2048 + lane * 16;
      *(LAS f4*)p = f4{a[r0], a[r0 + 1], a[r0 + 2], a[r0 + 3]};
      *(LAS f4*)(p + 1024) = f4{a[r0 + 4], a[r0 + 5], a[r0 + 6], a[r0 + 7]};
    } else {
#pragma unroll
      for (int e = 0; e < 8; ++e) o8[e] = a[r0 + e];
    }
  }
  VMW(0);
  BAR();
  STAMP(12);
#pragma unroll
  for (int s = 0; s < 3; ++s)
#pragma unroll
    for (int u = 0; u < 2; ++u) {
      const f4 v = *(LAS f4*)(S + 32768 + ((dh * 4 + q3) * 3 + s) * 2048 + u * 1024 + lane * 16);
#pragma unroll
      for (int e = 0; e < 4; ++e) o8[4 * u + e] += v[e];
    }
#pragma unroll
  for (int u = 0; u < 2; ++u) {
    const v2u yv = trd(S + YI_OFF + (dl + 8 * u + 4 * hh + trq) * 128 + (ll + 16 * trb + 4 * trp) * 2);
    v2u o;
#pragma unroll
    for (int e = 0; e < 2; ++e) {
      const unsigned m = pk(o8[4 * u + 2 * e], o8[4 * u + 2 * e + 1]);
      const unsigned mb = pk(bf_lo(m) + bf_lo(bo[u][e]), bf_hi(m) + bf_hi(bo[u][e]));
      const unsigned xy = pk(bf_lo(xo[u][e]) + bf_lo(yv[e]), bf_hi(xo[u][e]) + bf_hi(yv[e]));
      o[e] = pk(bf_lo(xy) + bf_lo(mb), bf_hi(xy) + bf_hi(mb));
    }
    *(GAS v2u*)((GAS char*)out + (size_t)(64 * lt + ll + r31) * 1024 + (64 * dt + dl + 8 * u + 4 * hh) * 2) = o;
  }
  VMW(0);
  STAMP(15);
  stv[14] = stv[0] + (ct() - c0_) / 10;
  if (b == 100 && wave == PROBEW) { const u64 tgt = stv[0] + 5000 + 10 * (stv[PROBE] - stv[0]); for (int i = 0; i < 1000000 && rt() < tgt; ++i) {} }
  if (lane == 0) {
    GAS u64* d = (GAS u64*)(ws + (32 << 20)) + (b * 8 + wave) * 16;
#pragma unroll
    for (int i = 0; i < 16; ++i) d[i] = stv[i];
  }
  asm volatile(".Lfk_end:" ::: "memory");
}

extern "C" void kernel_launch(void* const* d_in, const int* in_sizes, int n_in, void* d_out, int out_size, void* d_ws, size_t ws_size,
                              hipStream_t stream) {
  (void)hipFuncSetAttribute((const void*)fused_k, hipFuncAttributeMaxDynamicSharedMemorySize, LDS_BYTES);
  fused_k<<<256, 512, LDS_BYTES, stream>>>((const u16*)d_in[0], (const u16*)d_in[1], (const u16*)d_in[2], (const u16*)d_in[3], (const u16*)d_in[4],
                                           (const u16*)d_in[5], (const u16*)d_in[6], (const u16*)d_in[7], (const u16*)d_in[8], (const u16*)d_in[10],
                                           (const u16*)d_in[11], (const u16*)d_in[12], (const u16*)d_in[13], (u16*)d_out, (char*)d_ws);
}
```

```cpp
#include <hip/hip_runtime.h>
#include <stdint.h>

#define ABL_LAST "7"
#define GAPOFF 0
#define DI __device__ __forceinline__
#define LAS __attribute__((address_space(3)))
#define GAS __attribute__((address_space(1)))
#define CAS __attribute__((address_space(4)))
typedef unsigned short u16;
typedef unsigned long long u64;
typedef unsigned v4u __attribute__((ext_vector_type(4)));
typedef unsigned v2u __attribute__((ext_vector_type(2)));
typedef float f2 __attribute__((ext_vector_type(2)));
typedef float f4 __attribute__((ext_vector_type(4)));
typedef float f16v __attribute__((ext_vector_type(16)));
typedef __bf16 bf8 __attribute__((ext_vector_type(8)));
typedef __bf16 bf2 __attribute__((ext_vector_type(2)));
typedef short s4 __attribute__((ext_vector_type(4)));
typedef __amdgpu_buffer_rsrc_t rsrc_t;

constexpr int SL = 2048, DM = 512, NM = 128, FF = 1792;
constexpr int WS_E = 0, WS_F0 = 4096, WS_FH = 32768, WS_FY = 98304, WS_ST = 163840, WS_CS = 180224, WS_XT = 262144;
constexpr int WS_W2 = WS_XT + (2 << 20), WS_Y = WS_W2 + (2 << 20), WS_H = WS_Y + (2 << 20), WS_END = WS_H + SL * FF * 2;
constexpr int SA = 98304, TROW = 528, TAB = 59 * TROW;
constexpr int LN_OFF = SA + 40 * TROW, LN_ROW = 1088, W2S_OFF = SA + TAB + 40 * TROW;
constexpr int KC = 4160, KCH = 4 * KC, KK_OFF = 0, XI_OFF = 2 * KCH, XI = 4608, CX_OFF = XI_OFF + 2 * XI, YI_OFF = 131072;
constexpr int AP_OFF = 160640, BP_OFF = AP_OFF + 1024, CSP_OFF = BP_OFF + 1024, CNT_OFF = CSP_OFF + 1024, STP_OFF = CNT_OFF + 32, LDS_BYTES = 163840;
static_assert(SA + 2 * TAB <= AP_OFF && CX_OFF + 16384 <= 65536 && STP_OFF + 32 <= LDS_BYTES, "lds map");
constexpr int SC1 = 16;
constexpr int SPIN = 1 << 16;

DI float bf_lo(unsigned u) { return __builtin_bit_cast(float, u << 16); }
DI float bf_hi(unsigned u) { return __builtin_bit_cast(float, u & 0xffff0000u); }
DI unsigned pk(float a, float b) { f2 v = {a, b}; return __builtin_bit_cast(unsigned, __builtin_convertvector(v, bf2)); }
DI float rbf(float a) { return bf_lo(pk(a, a)); }
DI f16v mfma32(v4u a, v4u b, f16v c) { return __builtin_amdgcn_mfma_f32_32x32x16_bf16(__builtin_bit_cast(bf8, a), __builtin_bit_cast(bf8, b), c, 0, 0, 0); }
DI f4 mfma16(v4u a, v4u b, f4 c) { return __builtin_amdgcn_mfma_f32_16x16x32_bf16(__builtin_bit_cast(bf8, a), __builtin_bit_cast(bf8, b), c, 0, 0, 0); }
DI v2u trd(LAS char* p) { return __builtin_bit_cast(v2u, __builtin_amdgcn_ds_read_tr16_b64_v4i16((LAS s4*)p)); }
DI v4u cat(v2u a, v2u b) { return v4u{a.x, a.y, b.x, b.y}; }
#define LD128(p) (*(LAS v4u*)(p))
#define LD64(p) (*(LAS v2u*)(p))
#define LD32(p) (*(LAS unsigned*)(p))
#define LD16(p) (*(LAS u16*)(p))
#define SCHED() __builtin_amdgcn_sched_barrier(0)
#define BAR() do { SCHED(); asm volatile("" ::: "memory"); __builtin_amdgcn_s_waitcnt(0xC07F); asm volatile("s_barrier" ::: "memory"); SCHED(); } while (0)
#define LP(a) ((LAS char*)(size_t)(unsigned)(a))
#define VMW(n) asm volatile("s_waitcnt vmcnt(" #n ")" ::: "memory")
DI v4u mk_srd(const void* p, unsigned bytes) { u64 a = (u64)p; return v4u{(unsigned)a, (unsigned)(a >> 32) & 0xffffu, bytes, 0x00020000u}; }
DI rsrc_t mk_rsrc(const void* p, int bytes) { return __builtin_amdgcn_make_buffer_rsrc((void*)p, 0, bytes, 0x00020000); }
DI void dma(v4u srd, int voff, int soff, unsigned lds) {
  asm volatile("s_mov_b32 m0, %3\n\ts_nop 0\n\tbuffer_load_dwordx4 %0, %1, %2 offen lds" :: "v"(voff), "s"(srd), "s"(soff), "s"(lds) : "memory");
}
DI void dma_sc1(v4u srd, int voff, int soff, unsigned lds) {
  asm volatile("s_mov_b32 m0, %3\n\ts_nop 0\n\tbuffer_load_dwordx4 %0, %1, %2 offen sc1 lds" :: "v"(voff), "s"(srd), "s"(soff), "s"(lds) : "memory");
}
DI void dma4_sc1(v4u srd, int voff, int soff, unsigned lds) {
  asm volatile("s_mov_b32 m0, %3\n\ts_nop 0\n\tbuffer_load_dwordx4 %0, %1, %2 offen sc1 lds\n\tbuffer_load_dwordx4 %0, %1, %2 offen offset:1024 sc1 lds\n\t"
               "buffer_load_dwordx4 %0, %1, %2 offen offset:2048 sc1 lds\n\tbuffer_load_dwordx4 %0, %1, %2 offen offset:3072 sc1 lds"
               :: "v"(voff), "s"(srd), "s"(soff), "s"(lds) : "memory");
}
template <int C> DI float dpp_add(float v) { return v + __builtin_bit_cast(float, __builtin_amdgcn_update_dpp(0, __builtin_bit_cast(int, v), C, 0xf, 0xf, true)); }
DI float rl(float v, int l) { return __builtin_bit_cast(float, __builtin_amdgcn_readlane(__builtin_bit_cast(int, v), l)); }
DI float wave_sum(float v) {
  v = dpp_add<0xB1>(v); v = dpp_add<0x4E>(v); v = dpp_add<0x141>(v); v = dpp_add<0x140>(v);
  return (rl(v, 0) + rl(v, 16)) + (rl(v, 32) + rl(v, 48));
}
DI f2 cmul(f2 a, f2 b) { return f2{a.x * b.x - a.y * b.y, a.x * b.y + a.y * b.x}; }
DI f2 cexp_(float lr2, float lrev, float e) {
  float m = __builtin_amdgcn_exp2f(lr2 * e), a = __builtin_amdgcn_fractf(lrev * e);
  return f2{m * __builtin_amdgcn_cosf(a), m * __builtin_amdgcn_sinf(a)};
}
DI u64 ld64(char* ws, int off) { return __hip_atomic_load((GAS u64*)(ws + off), __ATOMIC_RELAXED, __HIP_MEMORY_SCOPE_AGENT); }
#define ORDER() __builtin_amdgcn_fence(__ATOMIC_ACQUIRE, "wavefront")
DI void st64(rsrc_t r, int off, unsigned lo, unsigned hi) { __builtin_amdgcn_raw_buffer_store_b64(v2u{lo, hi}, r, off, 0, SC1); }

#define STAMP(k) do {} while (0)
#define DET(v)
#define DETACC(a, b)

#define STB(k) do {} while (0)
#define P1UNROLL _Pragma("unroll 1")
#define LD64V(p) (*(volatile LAS v2u*)(p))
#define LD32V(p) (*(volatile LAS unsigned*)(p))
struct cz { float r, i, n; };
DI cz mkz(f2 z) { return cz{z.x, z.y, -z.y}; }
DI cz conj(cz z) { return cz{z.r, z.n, z.i}; }
DI f2 cm(f2 a, cz z) {
  f2 o;
  asm("v_mul_f32 %0, %2, %4\n\tv_mul_f32 %1, %2, %5\n\tv_fmac_f32 %0, %3, %6\n\tv_fmac_f32 %1, %3, %4" : "=&v"(o.x), "=&v"(o.y) : "v"(a.x), "v"(a.y), "v"(z.r), "v"(z.i), "v"(z.n));
  return o;
}
DI void step2(f2& v, cz zv, f2& u, cz zu, unsigned& pv, unsigned& pu) {
  f2 nv, nu;
  asm("v_mul_f32 %0, %6, %10\n\tv_mul_f32 %2, %8, %13\n\tv_mul_f32 %1, %6, %11\n\tv_mul_f32 %3, %8, %14\n\t"
      "v_cvt_pk_bf16_f32 %4, %6, %7\n\tv_fmac_f32 %0, %7, %12\n\tv_fmac_f32 %2, %9, %15\n\tv_cvt_pk_bf16_f32 %5, %8, %9\n\t"
      "v_fmac_f32 %1, %7, %10\n\tv_fmac_f32 %3, %9, %13"
      : "=&v"(nv.x), "=&v"(nv.y), "=&v"(nu.x), "=&v"(nu.y), "=&v"(pv), "=&v"(pu)
      : "v"(v.x), "v"(v.y), "v"(u.x), "v"(u.y), "v"(zv.r), "v"(zv.i), "v"(zv.n), "v"(zu.r), "v"(zu.i), "v"(zu.n));
  v = nv; u = nu;
}
DI bool tag_ok(u64 f, unsigned lo, unsigned hi) { return __all((unsigned)f == lo && (unsigned)(f >> 32) == hi); }

__device__ __forceinline__ unsigned long long rt2() { unsigned long long t; asm volatile("s_memrealtime %0\n\ts_waitcnt lgkmcnt(0)" : "=s"(t) :: "memory"); return t; }
__global__ void k_first(char* ws) { if (threadIdx.x == 0) *(unsigned long long*)(ws + (40 << 20)) = rt2(); }
__global__ void k_last(char* ws) { const unsigned long long tgt = *(volatile unsigned long long*)(ws + (40 << 20)) + 10000; for (int i = 0; i < 2000000 && rt2() < tgt; ++i) {} }
__global__ __launch_bounds__(512, 2) void fused_k(const u16* __restrict__ x, const u16* __restrict__ Are, const u16* __restrict__ Aim,
    const u16* __restrict__ Bre, const u16* __restrict__ Bim, const u16* __restrict__ Cre, const u16* __restrict__ Cim,
    const u16* __restrict__ logdt, const u16* __restrict__ Dskip, const u16* __restrict__ w1, const u16* __restrict__ b1,
    const u16* __restrict__ w2, const u16* __restrict__ b2, u16* __restrict__ out, char* __restrict__ ws) {
  extern __shared__ __attribute__((aligned(16))) char smem[];
  LAS char* const S = (LAS char*)smem;
  STAMP(0);
  const u64 t00 = rt2();
  const int tid = threadIdx.x, lane = tid & 63, wave = __builtin_amdgcn_readfirstlane(tid >> 6);
  const int b = blockIdx.x, xcd = b & 7, j = b >> 3, g8 = xcd * 32 + j;
  if (wave >= 4) __builtin_amdgcn_s_setprio(3);
  asm volatile("" :: "s"(x), "s"(Are), "s"(Aim), "s"(Bre), "s"(Bim), "s"(Cre), "s"(Cim), "s"(logdt), "s"(Dskip), "s"(w1), "s"(b1), "s"(w2), "s"(b2), "s"(out), "s"(ws));
  v4u xr;
  asm volatile("global_load_dwordx4 %0, %1, %2" : "=v"(xr) : "v"(g8 * 8192 + tid * 16), "s"(x) : "memory");
  const bool mainb = j < 28;
  const int pl = j >= 14 ? 1 : 0, pp = xcd * 2 + pl, fs = j - 14 * pl;
  const int lt = xcd * 4 + (j >> 3), dt = j & 7;
  const int ch0 = xcd * 64 + j * 2;
  const rsrc_t rw = mk_rsrc(ws, WS_END);
  const v4u sw_ = mk_srd(ws, WS_END);
  const int r31 = lane & 31, hh = lane >> 5, trq = (lane & 15) >> 2, trp = lane & 3, trb = (lane >> 4) & 1;
  const int q3 = wave & 3, dh = wave >> 2;
  v4u w2f[28];
  const int w2off = WS_W2 + ((2 * dt + dh) * 112 + 28 * q3) * 1024 + lane * 16;
  unsigned tlo, thi;
#define ROW_WAIT(n) float v_[8]; asm volatile("s_waitcnt vmcnt(" #n ")\n\tv_lshlrev_b32 %0, 16, %8\n\tv_and_b32 %1, 0xffff0000, %8\n\tv_lshlrev_b32 %2, 16, %9\n\tv_and_b32 %3, 0xffff0000, %9\n\t" \
    "v_lshlrev_b32 %4, 16, %10\n\tv_and_b32 %5, 0xffff0000, %10\n\tv_lshlrev_b32 %6, 16, %11\n\tv_and_b32 %7, 0xffff0000, %11" \
    : "=&v"(v_[0]), "=&v"(v_[1]), "=&v"(v_[2]), "=&v"(v_[3]), "=&v"(v_[4]), "=&v"(v_[5]), "=&v"(v_[6]), "=&v"(v_[7]) : "v"(xr[0]), "v"(xr[1]), "v"(xr[2]), "v"(xr[3]) : "memory")
  auto layernorm = [&](float (&v)[8]) {
    float s;
    s = ((v[0] + v[1]) + (v[2] + v[3])) + ((v[4] + v[5]) + (v[6] + v[7]));
    const float mf = wave_sum(s) * (1.f / DM);
    float q0 = 0.f, q1 = 0.f;
#pragma unroll
    for (int e = 0; e < 4; ++e) { float d = v[2 * e] - mf, f = v[2 * e + 1] - mf; q0 += d * d; q1 += f * f; }
    const float var = rbf(wave_sum(q0 + q1) * (1.f / DM));
    const float m = rbf(mf), sd = rbf(__builtin_amdgcn_sqrtf(rbf(var + 1e-5f))), rs = __builtin_amdgcn_rcpf(sd);
    v4u o;
#pragma unroll
    for (int e = 0; e < 4; ++e) { unsigned d = pk(v[2 * e] - m, v[2 * e + 1] - m); o[e] = pk(bf_lo(d) * rs, bf_hi(d) * rs); }
    LD128(S + LN_OFF + wave * LN_ROW + lane * 16) = o;
    if (lane == 0) LD32(S + STP_OFF + wave * 4) = pk(-m, sd);
  };
  const int hs = WS_H + (2 * lt * 112 + 28 * q3) * 1024, hl = q3 * 8192;

  if (wave < 4) {
    const int gw = wave, wf = gw & 1, wl = gw >> 1;
    if (mainb) {
      const v4u sx = mk_srd(x, SL * DM * 2), s1 = mk_srd(w1, DM * FF * 2);
      int vx[4], v1[4];
#pragma unroll
      for (int i = 0; i < 4; ++i) {
        vx[i] = (lane >> 3) * 1024 + (((lane & 7) ^ ((4 * (i & 1) + (lane >> 4)) & 7)) << 4) + i * 7168;
        v1[i] = (lane >> 4) * 3584 + (((lane & 15) ^ ((lane >> 4) << 2) ^ i) << 4) + i * 13312;
      }
      const int rot = (fs + 4 * pl + xcd) & 7;
      const int sx0 = (pp * 128 + 32 * gw) * 1024, s10 = 16 * gw * 3584 + fs * 256;
      auto issue8 = [&](int s) {
        const int ks = (s + rot) & 7;
        asm volatile("s_mov_b32 m0, %12\n\ts_nop 0\n\tbuffer_load_dwordx4 %0, %8, %10 offen lds\n\tbuffer_load_dwordx4 %1, %8, %10 offen offset:1024 lds\n\t"
                     "buffer_load_dwordx4 %2, %8, %10 offen offset:2048 lds\n\tbuffer_load_dwordx4 %3, %8, %10 offen offset:3072 lds\n\t"
                     "s_add_u32 m0, m0, 0x4000\n\ts_nop 0\n\tbuffer_load_dwordx4 %4, %9, %11 offen lds\n\tbuffer_load_dwordx4 %5, %9, %11 offen offset:1024 lds\n\t"
                     "buffer_load_dwordx4 %6, %9, %11 offen offset:2048 lds\n\tbuffer_load_dwordx4 %7, %9, %11 offen offset:3072 lds"
                     :: "v"(vx[0]), "v"(vx[1]), "v"(vx[2]), "v"(vx[3]), "v"(v1[0]), "v"(v1[1]), "v"(v1[2]), "v"(v1[3]), "s"(sx), "s"(s1),
                        "s"(sx0 + ks * 128), "s"(s10 + ks * 229376), "s"(s * 32768 + gw * 4096) : "memory", "scc");
      };
      issue8(0); issue8(1);
      SCHED();
      STAMP(13);
      f16v acc[2][2];
#pragma unroll
      for (int a = 0; a < 2; ++a)
#pragma unroll
        for (int c = 0; c < 2; ++c) {
#pragma unroll
          for (int i = 0; i < 16; ++i) acc[a][c][i] = 0.f;
          asm volatile("" : "+v"(acc[a][c]));
        }
      int xa[4], wa[4];
#pragma unroll
      for (int i = 0; i < 4; ++i) {
        const int ft = i >> 1, t = i & 1;
        xa[i] = (64 * wl + r31) * 128 + (((2 * i + hh) ^ ((r31 >> 1) & 7)) << 4);
        wa[i] = (8 * hh + 4 * t + trq) * 256 + (((4 * (2 * wf + ft) + 2 * trb + (trp >> 1)) ^ ((trq << 2) | ((2 * hh + t) & 3))) << 4) + 8 * (trp & 1);
        asm volatile("" : "+v"(xa[i]), "+v"(wa[i]));
      }
      ROW_WAIT(16);
      layernorm(v_);
      SCHED();
      issue8(2);
      v2u FA[2][4][4]; v4u FB[2][4][2];
      auto rd = [&](int set, int n, bool first = false) {
        const int ks = n / 6, i = n % 6;
        if (i == 2 || i == 3) FB[set][ks][i - 2] = LD128(LP(xa[ks]) + (i - 2) * 4096);
        else { const int k = i < 2 ? i : i - 2; FA[set][ks][k] = trd(LP(wa[k]) + 16384 + ks * 4096); }
      };
      VMW(16);
      STAMP(7);
      STB(1);
      BAR();
      STAMP(1);
#pragma unroll
      for (int n = 0; n < 24; ++n) rd(0, n, true);
      int slot = 0;
      auto stage = [&](int set, int s) {
        const int dsl = slot == 65536 ? -65536 : 32768;
        const int ks = (s + 3 + rot) & 7, so_x = sx0 + ks * 128, so_w = s10 + ks * 229376, m0v = slot + gw * 4096;
        STB(2 + s);
        DET(d0_);
        asm volatile("s_cmp_lt_i32 %0, 6\n\ts_cbranch_scc1 1f\n\ts_waitcnt vmcnt(0)\n1:\n\ts_waitcnt vmcnt(8)" :: "s"(s) : "memory", "scc");
        DET(d1_);
        BAR();
        DET(d2_);
        DETACC(5, 6);
#pragma unroll
        for (int n = 0; n < 16; ++n) {
          const int k = n >> 2, a = (n >> 1) & 1, c = n & 1;
          acc[a][c] = mfma32(cat(FA[set][k][2 * a], FA[set][k][2 * a + 1]), FB[set][k][c], acc[a][c]);
          SCHED();
          if (n == 0) {
#pragma unroll
            for (int i = 0; i < 4; ++i) wa[i] += dsl;
            xa[0] += dsl;
          }
          if (n >= 1 && n <= 12) { rd(set ^ 1, 2 * n - 2); rd(set ^ 1, 2 * n - 1); }
          if (n == 1 || n == 4 || n == 7) xa[n / 3 + 1] += dsl;
          if (n >= 8) {
            const int i = n & 3;
            if (n == 8) asm volatile("s_cmp_gt_i32 %4, " ABL_LAST "\n\ts_cbranch_scc1 1f\n\ts_mov_b32 m0, %3\n\ts_nop 0\n\tbuffer_load_dwordx4 %0, %1, %2 offen lds\n1:" :: "v"(vx[0]), "s"(sx), "s"(so_x), "s"(m0v), "s"(s + 3) : "memory", "scc");
            else if (n == 12) asm volatile("s_cmp_gt_i32 %4, " ABL_LAST "\n\ts_cbranch_scc1 1f\n\ts_mov_b32 m0, %3\n\ts_nop 0\n\tbuffer_load_dwordx4 %0, %1, %2 offen lds\n1:" :: "v"(v1[0]), "s"(s1), "s"(so_w), "s"(m0v + 16384), "s"(s + 3) : "memory", "scc");
            else asm volatile("s_cmp_gt_i32 %4, " ABL_LAST "\n\ts_cbranch_scc1 1f\n\tbuffer_load_dwordx4 %0, %1, %2 offen offset:%3 lds\n1:" :: "v"(n < 12 ? vx[i] : v1[i]), "s"(n < 12 ? sx : s1), "s"(n < 12 ? so_x : so_w), "i"(i * 1024), "s"(s + 3) : "memory", "scc");
          }
          SCHED();
        }
        slot += dsl;
      };
P1UNROLL
      for (int s = 0; s < 8; s += 2) { stage(0, s); stage(1, s + 1); }
      STB(10);
      STAMP(2);
      v4u Ap[2], Bp[2]; float sd[2];
#pragma unroll
      for (int i = 0; i < 2; ++i) {
        v2u a = LD64(S + AP_OFF + (64 * wf + 32 * i + r31) * 8), bb = LD64(S + BP_OFF + (64 * wl + 32 * i + r31) * 8);
        sd[i] = bf_hi(bb.y);
        Ap[i] = v4u{hh ? 0u : a.x, hh ? 0u : a.y, 0u, 0u};
        Bp[i] = v4u{hh ? 0u : bb.x, hh ? 0u : bb.y, 0u, 0u};
      }
#pragma unroll
      for (int c = 0; c < 2; ++c)
#pragma unroll
        for (int a = 0; a < 2; ++a) acc[a][c] = mfma32(Ap[a], Bp[c], acc[a][c]);
      STAMP(3);
#pragma unroll
      for (int c = 0; c < 2; ++c) {
        const float s = sd[c], rs = __builtin_amdgcn_rcpf(s);
        const float c0 = -2.3022081985f * rs, c1 = c0 * 0.044715f * rs * rs;
        const f2 c0v = {c0, c0}, c1v = {c1, c1}, sv = {s, s};
#pragma unroll
        for (int a = 0; a < 2; ++a)
#pragma unroll
          for (int u = 0; u < 2; ++u) {
            v4u o;
#pragma unroll
            for (int e = 0; e < 4; ++e) {
              f2 v = {acc[a][c][8 * u + 2 * e], acc[a][c][8 * u + 2 * e + 1]};
              f2 z = v * __builtin_elementwise_fma(v * v, c1v, c0v);
              f2 ex = {__builtin_amdgcn_exp2f(z.x), __builtin_amdgcn_exp2f(z.y)};
              f2 d = __builtin_elementwise_fma(ex, sv, sv);
              f2 r = {__builtin_amdgcn_rcpf(d.x), __builtin_amdgcn_rcpf(d.y)};
              f2 g = v * r;
              o[e] = pk(g.x, g.y);
            }
            __builtin_amdgcn_raw_buffer_store_b128(o, rw, lane * 16 + (WS_H + ((4 * pp + 2 * wl + c) * 112 + 8 * fs + 4 * wf + 2 * a + u) * 1024), 0, SC1);
          }
      }
    } else {
      const int sp = min(xcd * 4 + (j - 28), 27);
      float cs[8];
#pragma unroll
      for (int i = 0; i < 8; ++i) cs[i] = 0.f;
      const GAS char* p = (const GAS char*)w1 + (size_t)(gw * 128 + (lane >> 3)) * 3584 + sp * 128 + (lane & 7) * 16;
      v4u v[16];
#pragma unroll
      for (int i = 0; i < 16; ++i) v[i] = *(const GAS v4u*)(p + i * 8 * 3584);
      SCHED();
      ROW_WAIT(16);
      layernorm(v_);
      BAR();
      STAMP(1);
#pragma unroll
      for (int i = 0; i < 16; ++i)
#pragma unroll
        for (int e = 0; e < 4; ++e) { cs[2 * e] += bf_lo(v[i][e]); cs[2 * e + 1] += bf_hi(v[i][e]); }
#pragma unroll
      for (int i = 0; i < 8; ++i) { cs[i] += __shfl_xor(cs[i], 8); cs[i] += __shfl_xor(cs[i], 16); cs[i] += __shfl_xor(cs[i], 32); }
      if (lane < 8) {
        *(LAS f4*)(S + CSP_OFF + gw * 256 + lane * 32) = f4{cs[0], cs[1], cs[2], cs[3]};
        *(LAS f4*)(S + CSP_OFF + gw * 256 + lane * 32 + 16) = f4{cs[4], cs[5], cs[6], cs[7]};
      }
      BAR();
      if (xcd * 4 + (j - 28) < 28 && gw == 0) {
        const unsigned tl = LD32(S + CNT_OFF + 8);
        LAS float* c = (LAS float*)(S + CSP_OFF);
        float t = (c[lane] + c[64 + lane]) + (c[128 + lane] + c[192 + lane]);
        st64(rw, WS_CS + (sp * 64 + lane) * 8, __builtin_bit_cast(unsigned, t), tl);
      }
      STAMP(2);
#pragma unroll 1
      for (int s = 0; s < 7; ++s) BAR();
    }
    SCHED();
    if (lane == 0) LD32V(S + CNT_OFF + 16 + gw * 4) = 1u;
    { v2u t = LD64(S + CNT_OFF + 8); tlo = __builtin_amdgcn_readfirstlane(t.x); thi = __builtin_amdgcn_readfirstlane(t.y); }
    STAMP(4);
#pragma unroll 1
    for (int sp = 0; sp < SPIN && LD32V(S + CNT_OFF) < 4u; ++sp) __builtin_amdgcn_s_sleep(1);
    asm volatile("" ::: "memory");
    SCHED();
#pragma unroll
    for (int i = 0; i < 14; ++i) w2f[i] = __builtin_amdgcn_raw_buffer_load_b128(rw, w2off + i * 1024, 0, SC1);
    SCHED();
    VMW(14);
    if (mainb && lane == 0) st64(rw, WS_FH + (((pp * 2 + wl) * 14 + fs) * 2 + wf) * 64, tlo, thi);
    STAMP(5);
    SCHED();
#pragma unroll
    for (int i = 14; i < 28; ++i) w2f[i] = __builtin_amdgcn_raw_buffer_load_b128(rw, w2off + i * 1024, 0, SC1);
    STAMP(6);
    {
      const int fo = WS_FH + (lt * 28 + 7 * q3 + min(lane, 6)) * 64;
#pragma unroll 1
      for (int it = 0; it < SPIN; ++it) if (tag_ok(ld64(ws, fo), tlo, thi)) break;
    }
    STAMP(9);
#pragma unroll
    for (int t = 0; t < 2; ++t)
#pragma unroll
      for (int c = 0; c < 2; ++c) dma4_sc1(sw_, lane * 16, hs + c * 114688 + t * 4096, (t + 2) * 32768 + hl + c * 4096);
    BAR();
#pragma unroll
    for (int c = 0; c < 2; ++c) dma4_sc1(sw_, lane * 16, hs + c * 114688 + 2 * 4096, hl + c * 4096);
    VMW(16);
  } else {
    const int sw = wave - 4, cc = sw >> 1, sh = sw & 1, ch = ch0 + cc;
    const v2u ev = __builtin_amdgcn_raw_buffer_load_b64(rw, WS_E, 0, SC1);
    const int pi = ch * NM + 64 * sh + lane;
    const u16 p_ar = Are[pi], p_ai = Aim[pi], p_br = Bre[pi], p_bi = Bim[pi], p_cr = Cre[pi], p_ci = Cim[pi];
    SCHED();
    const int pid = (xcd * 28 + min(j, 27)) * 4 + sw;
    {
      const v4u s2 = mk_srd(w2, FF * DM * 2);
      const int so = (pid >> 3) * 16 * 1024 + (pid & 7) * 128, vo = (lane >> 3) * 1024 + (lane & 7) * 16;
      dma(s2, vo, so, W2S_OFF + sw * 2048);
      dma(s2, vo, so + 8192, W2S_OFF + sw * 2048 + 1024);
    }
    unsigned codew;
    {
      u64 pc; unsigned csz;
      asm volatile("s_getpc_b64 %0\n.Lfk_begin:\n\ts_mov_b32 %1, .Lfk_end-.Lfk_begin-4" : "=s"(pc), "=s"(csz));
      unsigned o = min((unsigned)(j * 8 + sw * 2 + (lane & 1)) * 128u, csz);
      asm volatile("global_load_dword %0, %1, %2" : "=v"(codew) : "v"(o), "s"(pc) : "memory");
    }
    __builtin_amdgcn_s_setprio(0);
    const unsigned ldt2 = *(const CAS unsigned*)(logdt + ch0), dsk2 = *(const CAS unsigned*)(Dskip + ch0);
    const float ldt = cc ? bf_hi(ldt2) : bf_lo(ldt2), dsk = cc ? bf_hi(dsk2) : bf_lo(dsk2);
    ROW_WAIT(10);
    STAMP(13);
    layernorm(v_);
    { u64 e = (((u64)ev.y << 32) | ev.x) + 1; tlo = __builtin_amdgcn_readfirstlane((unsigned)e); thi = __builtin_amdgcn_readfirstlane((unsigned)(e >> 32)); }
    if (tid == 256) { *(LAS v4u*)(S + CNT_OFF) = v4u{0u, 0u, tlo, thi}; *(LAS v4u*)(S + CNT_OFF + 16) = v4u{0u, 0u, 0u, 0u}; }
    STAMP(1);
    STB(1);
    BAR();
    VMW(0);
    asm volatile("" :: "v"(codew));
    if (tid >= 504) st64(rw, WS_ST + (g8 * 8 + (lane & 7)) * 8, LD32(S + STP_OFF + (lane & 7) * 4), tlo);
#pragma unroll
    for (int u = 0; u < 2; ++u) {
      LAS char* p = S + LN_OFF + trq * LN_ROW + (128 * sw + 64 * u + (lane & 48) + 4 * trp) * 2;
      v4u o = cat(trd(p), trd(p + 4 * LN_ROW));
      __builtin_amdgcn_raw_buffer_store_b128(o, rw, lane * 16 + (WS_XT + g8 * 8192 + (128 * sw + 64 * u) * 16), 0, SC1);
    }
    if (mainb) {
#pragma unroll
      for (int u = 0; u < 2; ++u) {
        LAS char* p = S + W2S_OFF + sw * 2048 + (4 * hh + trq) * 128 + (32 * u + 16 * trb + 4 * trp) * 2;
        v4u o = cat(trd(p), trd(p + 1024));
        __builtin_amdgcn_raw_buffer_store_b128(o, rw, lane * 16 + (WS_W2 + ((2 * (pid & 7) + u) * 112 + (pid >> 3)) * 1024), 0, SC1);
      }
    }
    STAMP(2);
    STB(2);
    BAR();
    const float dtv = rbf(__builtin_amdgcn_exp2f(ldt * 1.4426950408889634f));
    float lr2 = (dtv * bf_lo(p_ar)) * 1.4426950408889634f, lrev = (dtv * bf_lo(p_ai)) * 0.15915494309189535f;
    asm("" : "+v"(lr2));
    float dbr = dtv * bf_lo(p_br);
    asm("" : "+v"(dbr));
    const f2 cb = cm(f2{bf_lo(p_cr), -bf_lo(p_ci)}, conj(mkz(f2{dbr, dtv * bf_lo(p_bi)})));
    const cz z8 = mkz(cexp_(lr2, lrev, 8.f)), z8c = conj(z8), z1 = conj(mkz(cexp_(lr2, lrev, 1.f))), z256 = conj(mkz(cexp_(lr2, lrev, 256.f)));
    const cz z2 = mkz(cm(f2{z1.r, z1.i}, z1)), z4 = mkz(cm(f2{z2.r, z2.i}, z2));
    LAS char* const T = S + SA + cc * TAB + (64 * sh + lane) * 4;
    f2 V = {1.f, 0.f}, U = cb, W = cb;
    auto put = [&](int r, cz zu) {
 unsigned pv, pu; step2(V, z8, U, zu, pv, pu); LD32V(T + r * TROW) = pv; LD32V(T + (32 + r) * TROW) = pu; };
    STB(3);
    BAR();
#pragma unroll
    for (int i = 0; i < 6; ++i) put(i, z1);
    STB(4);
    BAR();
    put(6, z1); put(7, z1);
    W = cm(W, z256); U = W;
#pragma unroll
    for (int i = 0; i < 4; ++i) put(8 + i, z2);
    VMW(0);
    STB(5);
    BAR();
    if (tid == 256) st64(rw, WS_F0 + b * 64, tlo, thi);
    put(12, z2);
    W = cm(W, z256); U = W;
    put(13, z4); put(14, z4); put(15, z4);
    W = cm(W, z256); U = W;
    put(16, z4); put(17, z4);
    STB(6);
    BAR();
    const int goff = cc == 0 ? WS_ST + (pp * 128 + 64 * sh + lane) * 8 : WS_CS + (min(fs, 13) * 128 + 64 * sh + lane) * 8;
    const unsigned b1v = b1[min(fs, 13) * 128 + 64 * sh + lane];
    u64 gr = ld64(ws, goff);
    put(18, z4);
#pragma unroll
    for (int k = 0; k < 2; ++k) { W = cm(W, z256); U = W; put(19 + 2 * k, z8c); put(20 + 2 * k, z8c); }
    STB(7);
    BAR();
    const int pg = 64 * sw + lane;
    const int foff = WS_F0 + (((pg & 31) << 3) | (pg >> 5)) * 64;
    u64 f0 = ld64(ws, foff);
#pragma unroll
    for (int k = 2; k < 4; ++k) { W = cm(W, z256); U = W; put(19 + 2 * k, z8c); put(20 + 2 * k, z8c); }
    STB(8);
    BAR();
#pragma unroll
    for (int i = 27; i < 32; ++i) { LD32V(T + i * TROW) = pk(V.x, V.y); V = cm(V, z8); }
    if (mainb) {
#pragma unroll 1
      for (int sp = 0; sp < SPIN && !__all((unsigned)(gr >> 32) == tlo); ++sp) gr = ld64(ws, goff);
      const unsigned pv = (unsigned)gr;
      if (cc == 0) {
        const unsigned nm = pv & 0xffffu;
        LD64(S + BP_OFF + (64 * sh + lane) * 8) = v2u{nm | (nm << 16), pv};
      } else {
        const float c = __builtin_bit_cast(float, pv), c_hi = rbf(c), r1 = c - c_hi, c_mid = rbf(r1), r2 = r1 - c_mid;
        LD64(S + AP_OFF + (64 * sh + lane) * 8) = v2u{pk(c_hi, c_mid), (pk(r2, r2) & 0xffffu) | (b1v << 16)};
      }
    }
    STAMP(3);
    STB(9);
    BAR();
#pragma unroll 1
    for (int it = 0; it < SPIN && !tag_ok(f0, tlo, thi); ++it) f0 = ld64(ws, foff);
    ORDER();
    if (lane == 0) __hip_atomic_fetch_add((LAS unsigned*)(S + CNT_OFF + 4), 1u, __ATOMIC_RELAXED, __HIP_MEMORY_SCOPE_WORKGROUP);
    v4u xv[2];
#pragma unroll
    for (int u = 0; u < 2; ++u) xv[u] = __builtin_amdgcn_raw_buffer_load_b128(rw, (lane >> 1) * 8192 + (lane & 1) * 16, WS_XT + (64 * sw + 32 * u) * 8192 + ch0 * 16, SC1);
    auto ldw = [&](int i) { w2f[i] = __builtin_amdgcn_raw_buffer_load_b128(rw, w2off + (i & 3) * 1024, (i >> 2) * 4096, SC1); };
    f16v kk;
    {
      const int t = r31 & 7, bk = r31 >> 3;
      int ur;
      if (sh) ur = 19 + 2 * bk + (t >= 5 ? 1 : 0);
      else ur = bk == 0 ? t : (bk == 1 ? 8 + ((0x43222100u >> (4 * t)) & 15) : 7 + 3 * bk + ((0x22111000u >> (4 * t)) & 15));
      LAS char* pa = S + SA + cc * TAB + (32 + ur) * TROW + hh * 16;
      LAS char* pb = S + SA + cc * TAB + r31 * TROW + hh * 16;
#pragma unroll
      for (int i = 0; i < 16; ++i) kk[i] = 0.f;
      v4u A[16], B[16];
#pragma unroll
      for (int s = 0; s < 6; ++s) { A[s] = LD128(pa + s * 32); B[s] = LD128(pb + s * 32); }
#pragma unroll 1
      for (int sp = 0; sp < SPIN && LD32V(S + CNT_OFF + 4) < 4u; ++sp) __builtin_amdgcn_s_sleep(1);
      asm volatile("" ::: "memory");
      if (b == 0 && tid == 256) st64(rw, WS_E, tlo, thi);
      STAMP(4);
      SCHED();
#pragma unroll
      for (int s = 0; s < 16; ++s) {
        if (s + 6 < 16) { A[s + 6] = LD128(pa + (s + 6) * 32); B[s + 6] = LD128(pb + (s + 6) * 32); }
        kk = mfma32(A[s], B[s], kk);
        if (s < 12) ldw(s);
        SCHED();
      }
    }
    STAMP(5);
    {
      LAS char* kb = S + KK_OFF + cc * KCH + 8;
#pragma unroll
      for (int g = 0; g < 4; ++g) {
        float k0 = kk[4 * g], k1 = kk[4 * g + 1], k2 = kk[4 * g + 2], k3 = kk[4 * g + 3];
        if (sh && hh && (r31 & 1)) k0 = k1;
        const unsigned e01 = pk(k3, k2), e23 = pk(k1, k0), e12 = __builtin_amdgcn_alignbit(e23, e01, 16);
        LAS char* p = kb + (2044 - (256 * (4 * sh + 3) + 8 * r31 + 4 * hh)) * 2 + (3 - g) * 512;
        LD64(p) = v2u{e01, e23};
        LD16(p + KC - 2) = (u16)e01; LD32V(p + KC) = e12; LD16(p + KC + 4) = (u16)(e23 >> 16);
        LD32V(p + 2 * KC - 4) = e01; LD32V(p + 2 * KC) = e23;
        LD16(p + 3 * KC - 6) = (u16)e01; LD32V(p + 3 * KC - 4) = e12; LD16(p + 3 * KC) = (u16)(e23 >> 16);
        ldw(12 + 2 * g); ldw(13 + 2 * g);
        SCHED();
      }
      if (sh == 0) {
        const int s = lane >> 4, i = lane & 15;
        LD16(kb + s * KC + (2048 - s + i) * 2) = 0;
      } else if (lane < 30) {
        LD128(S + XI_OFF + cc * XI + lane * 16) = v4u{0u, 0u, 0u, 0u};
      }
#pragma unroll
      for (int u = 0; u < 2; ++u) LD128(S + XI_OFF + (lane & 1) * XI + 480 + (64 * sw + 32 * u + (lane >> 1)) * 16) = xv[u];
      asm volatile("s_waitcnt lgkmcnt(0)" ::: "memory");
      if (lane == 0) __hip_atomic_fetch_add((LAS unsigned*)(S + CNT_OFF), 1u, __ATOMIC_RELAXED, __HIP_MEMORY_SCOPE_WORKGROUP);
#pragma unroll
      for (int i = 20; i < 28; ++i) ldw(i);
      SCHED();
    }
#pragma unroll 1
    for (int sp = 0; sp < SPIN && LD32V(S + CNT_OFF) < 4u; ++sp) __builtin_amdgcn_s_sleep(1);
    asm volatile("" ::: "memory");
    STAMP(6);
    f4 ya[8];
#pragma unroll
    for (int i = 0; i < 8; ++i) ya[i] = f4{0.f, 0.f, 0.f, 0.f};
    {
      const int m = lane & 15, g = lane >> 4;
      LAS char* pa = S + KK_OFF + cc * KCH + (3 - (m & 3)) * KC + 8 + (2044 - 16 * (16 * 7 + 2 * (4 * sh + 3) + 1) - 4 * (m >> 2) + 8 * g) * 2;
      LAS char* pb = S + XI_OFF + cc * XI + (30 + 2 * m - 4 * (4 * sh + 3) - 2 + g) * 16;
      v4u A[4][8], B[4][8];
      auto lda = [&](int q, int p) { LAS char* a = pa + 512 * (7 - p) + 64 * (3 - q); A[q][p] = cat(LD64V(a), LD64V(a + 8)); };
      auto ldb = [&](int q, int e) { B[q][e] = *(volatile LAS v4u*)(pb + 512 * e + 64 * (3 - q)); };
      lda(0, 0);
#pragma unroll
      for (int e = 7; e >= 0; --e) ldb(0, e);
      lda(0, 1); lda(0, 2);
#pragma unroll 1
      for (int sp = 0; sp < SPIN && LD32V(S + CNT_OFF + 16 + sw * 4) == 0u; ++sp) __builtin_amdgcn_s_sleep(1);
      STAMP(9);
      SCHED();
#pragma unroll
      for (int q = 0; q < 4; ++q) {
        int n = 0;
#pragma unroll
        for (int p = 0; p < 8; ++p)
#pragma unroll
          for (int e = 7 - p; e >= 0; --e) {
            ya[p + e] = mfma16(A[q][p], B[q][e], ya[p + e]);
            if (n == 2) lda(q, 3);
            if (n == 8) lda(q, 4);
            if (n == 12) lda(q, 5);
            if (n == 16) lda(q, 6);
            if (n == 18) lda(q, 7);
            if (q < 3) {
              if (n == 20) lda(q + 1, 0);
              if (n == 28) lda(q + 1, 1);
              if (n == 34) lda(q + 1, 2);
              if (n == 4) ldb(q + 1, 7);
              if (n == 10) ldb(q + 1, 6);
              if (n == 17) ldb(q + 1, 5);
              if (n == 22) ldb(q + 1, 4);
              if (n == 24) ldb(q + 1, 3);
              if (n == 26) ldb(q + 1, 2);
              if (n == 30) ldb(q + 1, 1);
              if (n == 31) ldb(q + 1, 0);
            }
            SCHED();
            ++n;
          }
      }
    }
    f4 own[4];
#pragma unroll
    for (int i = 0; i < 4; ++i) {
      *(LAS f4*)(S + CX_OFF + sw * 4096 + i * 1024 + lane * 16) = sh ? ya[i] : ya[4 + i];
      own[i] = sh ? ya[4 + i] : ya[i];
    }
    STAMP(7);
    BAR();
    __builtin_amdgcn_s_waitcnt(0x0F70);
#pragma unroll
    for (int i = 0; i < 4; ++i) {
      const f4 y = own[i] + *(LAS f4*)(S + CX_OFF + (sw ^ 1) * 4096 + i * 1024 + lane * 16);
      const int l = 256 * (4 * sh + i) + 16 * (lane & 15) + 4 * (lane >> 4);
      const v2u xq = LD64(S + XI_OFF + cc * XI + 480 + l * 2);
      const unsigned y01 = pk(y[0], y[1]), y23 = pk(y[2], y[3]);
      const unsigned d01 = pk(bf_lo(xq.x) * dsk, bf_hi(xq.x) * dsk), d23 = pk(bf_lo(xq.y) * dsk, bf_hi(xq.y) * dsk);
      st64(rw, WS_Y + ch * 4096 + l * 2, pk(bf_lo(y01) + bf_lo(d01), bf_hi(y01) + bf_hi(d01)), pk(bf_lo(y23) + bf_lo(d23), bf_hi(y23) + bf_hi(d23)));
    }
  }
  STAMP(8);
  BAR();
  STAMP(10);
  const int hso = hs + dh * 114688, hlo = hl + dh * 4096;
  LAS char* const hp = S + hl + lane * 16;
  LAS char* hp2 = hp + 65536;
  asm volatile("" : "+v"(hp2));
  v4u H[2][8];
  auto ldh = [&](int t, int i) { const int sl = (t + 2) & 3; H[t & 1][i] = LD128((sl & 2 ? hp2 : hp) + (sl & 1) * 32768 + i * 1024); };
#pragma unroll
  for (int i = 0; i < 4; ++i) { ldh(0, i); ldh(0, 4 + i); }
  dma4_sc1(sw_, lane * 16, hso + 3 * 4096, 32768 + hlo);
  f16v acc[2];
#pragma unroll
  for (int c = 0; c < 2; ++c)
#pragma unroll
    for (int i = 0; i < 16; ++i) acc[c][i] = 0.f;
  const int dl = 32 * dh + 16 * (q3 & 1), ll = 32 * (q3 >> 1);
  const int fyo = WS_FY + ((lt >> 4) * 512 + 64 * dt + 8 * wave + (lane & 7)) * 64;
  u64 fy = 0;
  v2u xo[2], bo[2];
#pragma unroll
  for (int t = 0; t < 7; ++t) {
    if (t < 6) {
      DET(d0_);
      if (t == 0) VMW(12); else if (t < 4) VMW(8); else if (t == 4) VMW(4); else VMW(0);
      DET(d1_);
      BAR();
      DET(d2_);
      DETACC(3, 4);
      if (t == 2 && wave >= 4 && lane == 0) st64(rw, WS_FY + (((wave & 1) * 512) + ch0 + ((wave >> 1) & 1)) * 64, tlo, thi);
    }
    if (t == 5) {
#pragma unroll 1
      for (int it = 0; it < SPIN && !tag_ok(fy, tlo, thi); ++it) fy = ld64(ws, fyo);
      dma_sc1(sw_, (lane >> 3) * 4096 + (lane & 7) * 16, WS_Y + (64 * dt + 8 * wave) * 4096 + lt * 128, YI_OFF + wave * 1024);
    }
    SCHED();
#pragma unroll
    for (int i = 0; i < 4; ++i) {
      acc[0] = mfma32(w2f[4 * t + i], H[t & 1][i], acc[0]);
      SCHED();
      if (t < 6 && i < 2) { ldh(t + 1, 2 * i); ldh(t + 1, 4 + 2 * i); }
      SCHED();
      acc[1] = mfma32(w2f[4 * t + i], H[t & 1][4 + i], acc[1]);
      SCHED();
      if (t < 6 && i < 2) { ldh(t + 1, 2 * i + 1); ldh(t + 1, 5 + 2 * i); }
      SCHED();
      if (i == 2 && t <= 2) dma4_sc1(sw_, lane * 16, hso + (t + 4) * 4096, ((t + 2) & 3) * 32768 + hlo);
      if (i == 2 && t == 4) {
        fy = ld64(ws, fyo);
#pragma unroll
        for (int u = 0; u < 2; ++u) {
          xo[u] = *(const GAS v2u*)((const GAS char*)x + (size_t)(64 * lt + ll + r31) * 1024 + (64 * dt + dl + 8 * u + 4 * hh) * 2);
          bo[u] = *(const GAS v2u*)((const GAS char*)b2 + (64 * dt + dl + 8 * u + 4 * hh) * 2);
        }
      }
      SCHED();
    }
  }
  STAMP(11);
  float o8[8];
#pragma unroll
  for (int i = 0; i < 4; ++i) {
    const f16v& a = acc[i >> 1];
    const int r0 = 8 * (i & 1);
    if (i != q3) {
      LAS char* p = S + 32768 + ((dh * 4 + i) * 3 + ((q3 - i - 1) & 3)) * 2048 + lane * 16;
      *(LAS f4*)p = f4{a[r0], a[r0 + 1], a[r0 + 2], a[r0 + 3]};
      *(LAS f4*)(p + 1024) = f4{a[r0 + 4], a[r0 + 5], a[r0 + 6], a[r0 + 7]};
    } else {
#pragma unroll
      for (int e = 0; e < 8; ++e) o8[e] = a[r0 + e];
    }
  }
  VMW(0);
  BAR();
  STAMP(12);
#pragma unroll
  for (int s = 0; s < 3; ++s)
#pragma unroll
    for (int u = 0; u < 2; ++u) {
      const f4 v = *(LAS f4*)(S + 32768 + ((dh * 4 + q3) * 3 + s) * 2048 + u * 1024 + lane * 16);
#pragma unroll
      for (int e = 0; e < 4; ++e) o8[4 * u + e] += v[e];
    }
#pragma unroll
  for (int u = 0; u < 2; ++u) {
    const v2u yv = trd(S + YI_OFF + (dl + 8 * u + 4 * hh + trq) * 128 + (ll + 16 * trb + 4 * trp) * 2);
    v2u o;
#pragma unroll
    for (int e = 0; e < 2; ++e) {
      const unsigned m = pk(o8[4 * u + 2 * e], o8[4 * u + 2 * e + 1]);
      const unsigned mb = pk(bf_lo(m) + bf_lo(bo[u][e]), bf_hi(m) + bf_hi(bo[u][e]));
      const unsigned xy = pk(bf_lo(xo[u][e]) + bf_lo(yv[e]), bf_hi(xo[u][e]) + bf_hi(yv[e]));
      o[e] = pk(bf_lo(xy) + bf_lo(mb), bf_hi(xy) + bf_hi(mb));
    }
    *(GAS v2u*)((GAS char*)out + (size_t)(64 * lt + ll + r31) * 1024 + (64 * dt + dl + 8 * u + 4 * hh) * 2) = o;
  }
  asm volatile(".Lfk_end:" ::: "memory");
}

extern "C" void kernel_launch(void* const* d_in, const int* in_sizes, int n_in, void* d_out, int out_size, void* d_ws, size_t ws_size,
                              hipStream_t stream) {
  (void)hipFuncSetAttribute((const void*)fused_k, hipFuncAttributeMaxDynamicSharedMemorySize, LDS_BYTES);
  k_first<<<1, 64, 0, stream>>>((char*)d_ws);
  fused_k<<<256, 512, LDS_BYTES, stream>>>((const u16*)d_in[0], (const u16*)d_in[1], (const u16*)d_in[2], (const u16*)d_in[3], (const u16*)d_in[4],
                                           (const u16*)d_in[5], (const u16*)d_in[6], (const u16*)d_in[7], (const u16*)d_in[8], (const u16*)d_in[10],
                                           (const u16*)d_in[11], (const u16*)d_in[12], (const u16*)d_in[13], (u16*)d_out, (char*)d_ws);
  k_last<<<1, 64, 0, stream>>>((char*)d_ws);
}
```

```cpp
#define PPOS 1
#include <hip/hip_runtime.h>
#include <stdint.h>
#include <utility>

#define ABL_LAST "7"
#define ROT (2 * (xcd & 3) + 4 * (xcd >> 2))
#define PRIO_EARLY 1
#define PF_N 2
#define PF_G0 0
#define PF_ROLE 0
#define XT_FLAG_AT "3"
#define POLL_AT 5
#define YF 4
#define W2T_AT 1
#define SP_AT 20
#define KORD 0
#define CS_W (KORD ? 8 : 128)
#define CS_I (KORD ? 32 : 8)
#define PULLM 1
#define PULLS 0
#define PULLG 0
#define W2T_SP 2
#define HPOLL 22
#define GROT 0
#define GPRE 8
#define R2E 0
#define PMAP 1
#define TLBW 0
#define DI __device__ __forceinline__
#define LAS __attribute__((address_space(3)))
#define GAS __attribute__((address_space(1)))
#define CAS __attribute__((address_space(4)))
typedef unsigned short u16;
typedef unsigned long long u64;
typedef unsigned v4u __attribute__((ext_vector_type(4)));
typedef unsigned v2u __attribute__((ext_vector_type(2)));
typedef float f2 __attribute__((ext_vector_type(2)));
typedef float f4 __attribute__((ext_vector_type(4)));
typedef float f16v __attribute__((ext_vector_type(16)));
typedef __bf16 bf8 __attribute__((ext_vector_type(8)));
typedef __bf16 bf2 __attribute__((ext_vector_type(2)));
typedef short s4 __attribute__((ext_vector_type(4)));
typedef __amdgpu_buffer_rsrc_t rsrc_t;

constexpr int SL = 2048, DM = 512, NM = 128, FF = 1792;
constexpr int WS_E = 0, WS_F0 = 4096, WS_ST = 163840, WS_CS = 180224, WS_XT = 262144;
constexpr int WS_W2 = WS_XT + (2 << 20), WS_FH = WS_W2, WS_FY = WS_W2 + (1 << 20), WS_Y = WS_W2 + (2 << 20), WS_H = WS_Y + (2 << 20), WS_END = WS_H + SL * FF * 2;
constexpr int TROW = 528, TAB = 59 * TROW, TB_OFF = 0;
constexpr int RD_W = 16384;
constexpr int KC = 4160, KCH = 2 * KC, KK_OFF = 4 * RD_W, XI_OFF = KK_OFF + 2 * KCH, XI = 4608;
constexpr int ST_OFF = 98304, ST_W = 14336, LN_ROW = 320;
constexpr int XI_OFF2 = ST_OFF + 4 * ST_W;
constexpr int YI_OFF = 8 * RD_W, Y2_OFF = YI_OFF + 8192, Y2_ROW = 144;
constexpr int AP_OFF = 160640, BP_OFF = AP_OFF + 1024, CSP_OFF = BP_OFF + 1024, CNT_OFF = CSP_OFF + 1024, LDS_BYTES = 163840;
static_assert(2 * TAB <= KK_OFF && XI_OFF + 2 * XI + 4096 <= ST_OFF && ST_OFF + 4 * ST_W <= AP_OFF && Y2_OFF + 64 * Y2_ROW <= AP_OFF && CNT_OFF + 64 <= LDS_BYTES, "lds map");
#define FS0 32
#define FSH 128
#define FSY 128
constexpr int SC1 = 16;
constexpr int SPIN = 1 << 16;

DI float bf_lo(unsigned u) { return __builtin_bit_cast(float, u << 16); }
DI float bf_hi(unsigned u) { return __builtin_bit_cast(float, u & 0xffff0000u); }
DI unsigned pk(float a, float b) { f2 v = {a, b}; return __builtin_bit_cast(unsigned, __builtin_convertvector(v, bf2)); }
DI float rbf(float a) { return bf_lo(pk(a, a)); }
DI f16v mfma32(v4u a, v4u b, f16v c) { return __builtin_amdgcn_mfma_f32_32x32x16_bf16(__builtin_bit_cast(bf8, a), __builtin_bit_cast(bf8, b), c, 0, 0, 0); }
DI f4 mfma16(v4u a, v4u b, f4 c) { return __builtin_amdgcn_mfma_f32_16x16x32_bf16(__builtin_bit_cast(bf8, a), __builtin_bit_cast(bf8, b), c, 0, 0, 0); }
DI v2u trd(LAS char* p) { return __builtin_bit_cast(v2u, __builtin_amdgcn_ds_read_tr16_b64_v4i16((LAS s4*)p)); }
DI v4u cat(v2u a, v2u b) { return v4u{a.x, a.y, b.x, b.y}; }
#define LD128(p) (*(LAS v4u*)(p))
#define LD64(p) (*(LAS v2u*)(p))
#define LD32(p) (*(LAS unsigned*)(p))
#define LD16(p) (*(LAS u16*)(p))
#define SCHED() __builtin_amdgcn_sched_barrier(0)
#define BAR() do { SCHED(); asm volatile("" ::: "memory"); __builtin_amdgcn_s_waitcnt(0xC07F); asm volatile("s_barrier" ::: "memory"); SCHED(); } while (0)
#define LGKM0() do { SCHED(); asm volatile("" ::: "memory"); __builtin_amdgcn_s_waitcnt(0xC07F); SCHED(); } while (0)
#define BARM() do { if (mainb) BAR(); } while (0)
#define LP(a) ((LAS char*)(size_t)(unsigned)(a))
#define VMW(n) asm volatile("s_waitcnt vmcnt(" #n ")" ::: "memory")
DI v4u mk_srd(const void* p, unsigned bytes) { u64 a = (u64)p; return v4u{(unsigned)a, (unsigned)(a >> 32) & 0xffffu, bytes, 0x00020000u}; }
DI rsrc_t mk_rsrc(const void* p, int bytes) { return __builtin_amdgcn_make_buffer_rsrc((void*)p, 0, bytes, 0x00020000); }
DI void dma(v4u srd, int voff, int soff, unsigned lds) {
  asm volatile("s_mov_b32 m0, %3\n\ts_nop 0\n\tbuffer_load_dwordx4 %0, %1, %2 offen lds" :: "v"(voff), "s"(srd), "s"(soff), "s"(lds) : "memory");
}
DI void dma_lo(v4u srd, int voff, int soff, unsigned lds) {
  asm volatile("s_mov_b32 m0, %3\n\ts_setprio 0\n\tbuffer_load_dwordx4 %0, %1, %2 offen lds\n\ts_setprio 2" :: "v"(voff), "s"(srd), "s"(soff), "s"(lds) : "memory");
}
DI void dma_sc1(v4u srd, int voff, int soff, unsigned lds) {
  asm volatile("s_mov_b32 m0, %3\n\ts_nop 0\n\tbuffer_load_dwordx4 %0, %1, %2 offen sc1 lds" :: "v"(voff), "s"(srd), "s"(soff), "s"(lds) : "memory");
}
DI void dma4_sc1(v4u srd, int voff, int soff, unsigned lds) {
  asm volatile("s_mov_b32 m0, %3\n\ts_nop 0\n\tbuffer_load_dwordx4 %0, %1, %2 offen sc1 lds\n\tbuffer_load_dwordx4 %0, %1, %2 offen offset:1024 sc1 lds\n\t"
               "buffer_load_dwordx4 %0, %1, %2 offen offset:2048 sc1 lds\n\tbuffer_load_dwordx4 %0, %1, %2 offen offset:3072 sc1 lds"
               :: "v"(voff), "s"(srd), "s"(soff), "s"(lds) : "memory");
}
template <int C> DI float dpp_add(float v) { return v + __builtin_bit_cast(float, __builtin_amdgcn_update_dpp(0, __builtin_bit_cast(int, v), C, 0xf, 0xf, true)); }
DI float rl(float v, int l) { return __builtin_bit_cast(float, __builtin_amdgcn_readlane(__builtin_bit_cast(int, v), l)); }
DI float wave_sum(float v) {
  v = dpp_add<0xB1>(v); v = dpp_add<0x4E>(v); v = dpp_add<0x141>(v); v = dpp_add<0x140>(v);
  return (rl(v, 0) + rl(v, 16)) + (rl(v, 32) + rl(v, 48));
}
DI float swap_sum16(float v) {
  const unsigned w = __builtin_bit_cast(unsigned, v);
  auto r = __builtin_amdgcn_permlane16_swap(w, w, false, false);
  const unsigned r0 = r[0], r1 = r[1];
  return __builtin_bit_cast(float, r0) + __builtin_bit_cast(float, r1);
}
DI float swap_sum32(float v) {
  const unsigned w = __builtin_bit_cast(unsigned, v);
  auto r = __builtin_amdgcn_permlane32_swap(w, w, false, false);
  const unsigned r0 = r[0], r1 = r[1];
  return __builtin_bit_cast(float, r0) + __builtin_bit_cast(float, r1);
}
DI float half_sum(float v) {
  v = dpp_add<0xB1>(v); v = dpp_add<0x4E>(v); v = dpp_add<0x141>(v); v = dpp_add<0x140>(v);
  return swap_sum16(v);
}
DI f2 cmul(f2 a, f2 b) { return f2{a.x * b.x - a.y * b.y, a.x * b.y + a.y * b.x}; }
DI f2 cexp_(float lr2, float lrev, float e) {
  float m = __builtin_amdgcn_exp2f(lr2 * e), a = __builtin_amdgcn_fractf(lrev * e);
  return f2{m * __builtin_amdgcn_cosf(a), m * __builtin_amdgcn_sinf(a)};
}
DI u64 ld64(char* ws, int off) { return __hip_atomic_load((GAS u64*)(ws + off), __ATOMIC_RELAXED, __HIP_MEMORY_SCOPE_AGENT); }
#define ORDER() __builtin_amdgcn_fence(__ATOMIC_ACQUIRE, "wavefront")
DI void st64(rsrc_t r, int off, unsigned lo, unsigned hi) { __builtin_amdgcn_raw_buffer_store_b64(v2u{lo, hi}, r, off, 0, SC1); }

#define STAMP(k) do {} while (0)
#define DET(v)
#define DETACC(a, b)

#define STB(k) do {} while (0)
#define P1UNROLL _Pragma("unroll 1")
#define ST4(k) do {} while (0)
#define LD64V(p) (*(volatile LAS v2u*)(p))
#define LD32V(p) (*(volatile LAS unsigned*)(p))
struct cz { float r, i, n; };
DI cz mkz(f2 z) { return cz{z.x, z.y, -z.y}; }
DI cz conj(cz z) { return cz{z.r, z.n, z.i}; }
DI f2 cm(f2 a, cz z) {
  f2 o;
  asm("v_mul_f32 %0, %2, %4\n\tv_mul_f32 %1, %2, %5\n\tv_fmac_f32 %0, %3, %6\n\tv_fmac_f32 %1, %3, %4" : "=&v"(o.x), "=&v"(o.y) : "v"(a.x), "v"(a.y), "v"(z.r), "v"(z.i), "v"(z.n));
  return o;
}
DI void step2(f2& v, cz zv, f2& u, cz zu, unsigned& pv, unsigned& pu) {
  f2 nv, nu;
  asm("v_mul_f32 %0, %6, %10\n\tv_mul_f32 %2, %8, %13\n\tv_mul_f32 %1, %6, %11\n\tv_mul_f32 %3, %8, %14\n\t"
      "v_cvt_pk_bf16_f32 %4, %6, %7\n\tv_fmac_f32 %0, %7, %12\n\tv_fmac_f32 %2, %9, %15\n\tv_cvt_pk_bf16_f32 %5, %8, %9\n\t"
      "v_fmac_f32 %1, %7, %10\n\tv_fmac_f32 %3, %9, %13"
      : "=&v"(nv.x), "=&v"(nv.y), "=&v"(nu.x), "=&v"(nu.y), "=&v"(pv), "=&v"(pu)
      : "v"(v.x), "v"(v.y), "v"(u.x), "v"(u.y), "v"(zv.r), "v"(zv.i), "v"(zv.n), "v"(zu.r), "v"(zu.i), "v"(zu.n));
  v = nv; u = nu;
}
template <int I> struct ic { static constexpr int v = I; };
template <int... I, class F> DI void sfor_(std::integer_sequence<int, I...>, F&& f) { (f(ic<I>{}), ...); }
template <int N, class F> DI void sfor(F&& f) { sfor_(std::make_integer_sequence<int, N>{}, f); }
DI bool tag_ok(u64 f, unsigned lo, unsigned hi) { return __all((unsigned)f == lo && (unsigned)(f >> 32) == hi); }

__global__ __launch_bounds__(512, 2) void fused_k(const u16* __restrict__ x, const u16* __restrict__ Are, const u16* __restrict__ Aim,
    const u16* __restrict__ Bre, const u16* __restrict__ Bim, const u16* __restrict__ Cre, const u16* __restrict__ Cim,
    const u16* __restrict__ logdt, const u16* __restrict__ Dskip, const u16* __restrict__ w1, const u16* __restrict__ b1,
    const u16* __restrict__ w2, const u16* __restrict__ b2, u16* __restrict__ out, char* __restrict__ ws) {
  extern __shared__ __attribute__((aligned(16))) char smem[];
  LAS char* const S = (LAS char*)smem;
  STAMP(0);
  const int tid = threadIdx.x, wave0 = __builtin_amdgcn_readfirstlane(tid >> 6);
  const int b0 = blockIdx.x;
  const bool mainb = b0 < 224;
  unsigned tlo, thi, stp, aS;
  int xa[4], wa[4];
  if (wave0 < 4) {
    const int ln = tid & 63, xc = b0 & 7, jj = b0 >> 3, rows = (((xc & 3) * 2 + (xc >> 2)) * 32 + jj) * 8 + (tid >> 5);
    v4u xr[2]; u64 ev, pc; unsigned csz;
    asm volatile("s_getpc_b64 %0\n.Lfk_begin:\n\ts_mov_b32 %1, .Lfk_end-.Lfk_begin-4" : "=s"(pc), "=s"(csz));
    asm volatile("global_load_dwordx4 %0, %3, %8\n\tglobal_load_dwordx4 %1, %3, %8 offset:512\n\tglobal_load_dwordx2 %2, %4, %9 sc1\n\t"
                 "s_mov_b32 m0, %12\n\ts_nop 0\n\tglobal_load_lds_dword %5, %10\n\tglobal_load_lds_dword %6, %11\n\tglobal_load_lds_dword %7, %9 sc1"
                 : "=&v"(xr[0]), "=&v"(xr[1]), "=&v"(ev)
                 : "v"(rows * 1024 + (tid & 31) * 16), "v"(WS_E), "v"(min((unsigned)(jj * 24 + wave0 * 6 + min(ln, 5)) * 64u, csz)),
                   "v"((((xc & 3) * 32 + jj) * 4 + wave0) * 3584 + (xc >> 2) * 1792 + min(ln, KORD == 1 ? 0 : 27) * 64), "v"(min((ln & 1) + 2 * wave0, 6) * 0x200000 + 0x40000),
                   "s"(x), "s"(ws), "s"(pc), "s"(w1), "s"(XI_OFF2) : "memory");
    unsigned aL = ST_OFF + ((tid >> 4) & 1) * ST_W + (tid >> 5) * LN_ROW + (tid & 15) * 16;
    aS = WS_ST + rows * 8;
    asm volatile("" : "+v"(aL), "+v"(aS));
    {
      const int r31 = ln & 31, hh = ln >> 5, trq = (ln & 15) >> 2, trp = ln & 3, trb = (ln >> 4) & 1, wf = wave0 & 1, wl = wave0 >> 1;
#pragma unroll
      for (int i = 0; i < 4; ++i) {
        const int ft = i >> 1, t = i & 1;
        xa[i] = (64 * wl + r31) * 128 + (((2 * i + hh) ^ ((r31 >> 1) & 7)) << 4);
        wa[i] = (8 * hh + 4 * t + trq) * 256 + (((4 * (2 * wf + ft) + 2 * trb + (trp >> 1)) ^ ((trq << 2) | ((2 * hh + t) & 3))) << 4) + 8 * (trp & 1);
        asm volatile("" : "+v"(xa[i]), "+v"(wa[i]));
      }
    }
    asm volatile("s_waitcnt vmcnt(3)" : "+v"(xr[0]), "+v"(xr[1]), "+v"(ev) :: "memory");
    STAMP(13);
    f2 v[8];
#pragma unroll
    for (int e = 0; e < 8; ++e) v[e] = f2{bf_lo(xr[e >> 2][e & 3]), bf_hi(xr[e >> 2][e & 3])};
    f2 s2 = ((v[0] + v[1]) + (v[2] + v[3])) + ((v[4] + v[5]) + (v[6] + v[7]));
    const float mf = half_sum(s2.x + s2.y) * (1.f / DM);
    f2 q0 = {0.f, 0.f}, q1 = {0.f, 0.f};
#pragma unroll
    for (int e = 0; e < 4; ++e) { f2 d = v[2 * e] - mf, f = v[2 * e + 1] - mf; q0 = __builtin_elementwise_fma(d, d, q0); q1 = __builtin_elementwise_fma(f, f, q1); }
    q0 += q1;
    const float var = rbf(half_sum(q0.x + q0.y) * (1.f / DM));
    const float m = rbf(mf), sd = rbf(__builtin_amdgcn_sqrtf(rbf(var + 1e-5f))), rs = __builtin_amdgcn_rcpf(sd);
#pragma unroll
    for (int i = 0; i < 2; ++i) {
      v4u o;
#pragma unroll
      for (int e = 0; e < 4; ++e) { f2 c = v[4 * i + e] - m; unsigned d = pk(c.x, c.y); f2 n = f2{bf_lo(d), bf_hi(d)} * rs; o[e] = pk(n.x, n.y); }
      LD128(LP(aL) + 2 * i * ST_W) = o;
    }
    const u64 e = ev + 1;
    tlo = __builtin_amdgcn_readfirstlane((unsigned)e); thi = __builtin_amdgcn_readfirstlane((unsigned)(e >> 32));
    stp = pk(-m, sd);
  }
  int wave = wave0;
  asm volatile("" : "+s"(wave));
  const int q3 = wave & 3, dh = wave >> 2;
  int lane, b, xcd, j, g8, pp, fs, lt, dt, ch0, r31, hh, trq, trp, trb, v2o, s20, hv, fo, go[7];
  unsigned stg, stg2, aW, aW2;
  rsrc_t rw; v4u sw_, s2;
  auto late = [&]() {
    SCHED();
    lane = tid & 63; b = b0;
    char* wsp = ws; const u16* w2p = w2;
    asm volatile("" : "+v"(lane));
    asm volatile("" : "+s"(b), "+s"(wsp), "+s"(w2p));
    xcd = b & 7; j = b >> 3;
    const int rg = (xcd & 3) * 2 + (xcd >> 2);
    g8 = rg * 32 + j;
    pp = (xcd & 3) * 4 + (j & 3); fs = (xcd >> 2) * 7 + min(j >> 2, 6);
    {
      const int ds = ((0x180u - (unsigned)(u64)w2p) >> 7) & 7, c = j >> 2;
      lt = rg * 4 + (j & 3); dt = c == 7 ? ds : c + (c >= ds ? 1 : 0);
    }
    ch0 = xcd * 64 + j * 2;
    r31 = lane & 31; hh = lane >> 5; trq = (lane & 15) >> 2; trp = lane & 3; trb = (lane >> 4) & 1;
    rw = mk_rsrc(wsp, WS_END); sw_ = mk_srd(wsp, WS_END); s2 = mk_srd(w2p, FF * DM * 2);
    stg = dh ? ST_OFF + q3 * ST_W : q3 * RD_W;
    stg2 = ST_OFF + q3 * ST_W;
    v2o = (lane >> 3) * 1024 + (((lane & 7) ^ ((lane >> 2) & 4)) << 4); s20 = (448 * q3 + 32 * dh) * 1024 + dt * 128;
    aW = stg + (4 * hh + trq) * 128 + ((2 * trb + (trp >> 1)) ^ ((trq & 2) << 1)) * 16 + (trp & 1) * 8;
    aW2 = aW - stg + stg2;
#pragma unroll
    for (int g = 0; g < 7; ++g) { const int t = g + (GROT ? min(xcd, 6) : 0); go[g] = (t >= 7 ? t - 7 : t) * 4096; }
    hv = WS_H + ((2 * lt + dh) * 112 + 28 * q3) * 1024 + lane * 16;
    fo = WS_FH + (lt * 64 + 32 * dh + 7 * q3 + min(lane, 6)) * FSH;
  };
  v4u w2f[28];
  auto dmaw = [&](int n, bool lo = false) {
    SCHED();
    (lo ? dma_lo : dma)(s2, v2o, s20 + go[n >> 2] * 16 + (n & 3) * 8192, (n < 14 ? stg : stg2) + (n % 14) * 1024);
    SCHED();
  };
  auto trw = [&](int i) {
    const int o = ((i >> 1) % 7) * 2048;
    const unsigned a = (i < 14 ? aW : aW2) ^ ((i & 1) << 6);
    w2f[i] = cat(trd(LP(a) + o), trd(LP(a) + o + 1024));
  };
  constexpr int HW = 14;
  v4u H[28];
  auto ldh = [&](int i) { SCHED(); H[i] = __builtin_amdgcn_raw_buffer_load_b128(rw, hv + (i & 3) * 1024, go[i >> 2], SC1); SCHED(); };
  if (wave < 4) {
    const int gw = wave, wf = gw & 1, wl = gw >> 1;
    v4u xt[2];
    auto xt_in = [&]() {
#pragma unroll
      for (int u = 0; u < 2; ++u) {
        LAS char* p = S + ST_OFF + gw * ST_W + trq * LN_ROW + (64 * u + (lane & 48) + 4 * trp) * 2;
        xt[u] = cat(trd(p), trd(p + 4 * LN_ROW));
      }
    };
    auto xt_out = [&]() {
      st64(rw, r31 == 0 ? (int)aS : WS_END, stp, tlo);
#pragma unroll
      for (int u = 0; u < 2; ++u) __builtin_amdgcn_raw_buffer_store_b128(xt[u], rw, lane * 16 + (WS_XT + g8 * 8192 + (128 * gw + 64 * u) * 16), 0, SC1);
    };
    if (mainb) {
      asm volatile("; main" ::: "memory");
      f16v acc[2][2];
      v2u FA[4][4]; v4u FB[4][2];
      auto rd = [&](int n) {
        const int ks = n / 6, i = n % 6;
        if (i == 2 || i == 3) FB[ks][i - 2] = LD128(LP(xa[ks]) + (i - 2) * 4096);
        else { const int k = i < 2 ? i : i - 2; FA[ks][k] = trd(LP(wa[k]) + 16384 + ks * 4096); }
      };
      STB(1);
      BAR();
      STAMP(1);
#pragma unroll
      for (int n = 0; n < 12; ++n) { rd(n); SCHED(); }
      late();
      int f0o = WS_F0 + (gw * 256 + g8) * FS0;
      v2u tag2 = {tlo, thi};
      int fha = WS_FH + ((pp * 2 + wl) * 64 + fs * 2 + wf) * FSH, aD = CNT_OFF + 16 + gw * 4;
      asm volatile("" : "+v"(f0o), "+v"(tag2), "+v"(fha), "+v"(aD));
      int slot = 0;
      v4u Ap[2], Bp[2]; v2u pa_[2], pb_[2]; float sd[2]; unsigned apa, bpa;
      auto stage = [&](int s, bool more, bool first = false) {
        const int dsl = slot == 65536 ? -65536 : 32768;
#pragma unroll
        for (int n = 0; n < 16; ++n) {
          const int k = n >> 2, a = (n >> 1) & 1, c = n & 1;
          if (!more && n == 7) { apa = AP_OFF + (64 * wf + r31) * 8; bpa = BP_OFF + (64 * wl + r31) * 8; asm volatile("" : "+v"(apa), "+v"(bpa)); }
          if (n == 8) { STB(2 + s); BAR(); }
          if (!more && n == 8) { pa_[0] = LD64V(LP(apa)); pb_[0] = LD64V(LP(bpa)); pa_[1] = LD64V(LP(apa) + 256); pb_[1] = LD64V(LP(bpa) + 256); SCHED(); }
          if (first && n < 4) {
#pragma unroll
            for (int e = 0; e < 16; ++e) acc[a][c][e] = 0.f;
          }
          acc[a][c] = mfma32(cat(FA[k][2 * a], FA[k][2 * a + 1]), FB[k][c], acc[a][c]);
          SCHED();
          if (n < 4) { rd(12 + 3 * n); rd(13 + 3 * n); rd(14 + 3 * n); }
          if (first && n == 4) xt_in();
          if (first && n == 7) xt_out();
          if (n == 5) { wa[0] += dsl; wa[1] += dsl; wa[2] += dsl; wa[3] += dsl; }
          if (n == 6) { xa[0] += dsl; xa[1] += dsl; xa[2] += dsl; xa[3] += dsl; }
          if (more && n >= 8 && n < 14) { rd(2 * n - 16); rd(2 * n - 15); }
          if (!more && (n == 13 || n == 14)) {
            const int i = n - 13;
            sd[i] = bf_hi(pb_[i].y);
            Ap[i] = v4u{hh ? 0u : pa_[i].x, hh ? 0u : pa_[i].y, 0u, 0u};
            Bp[i] = v4u{hh ? 0u : pb_[i].x, hh ? 0u : pb_[i].y, 0u, 0u};
            asm volatile("" : "+v"(Ap[i]), "+v"(Bp[i]));
          }
          SCHED();
        }
        slot += dsl;
      };
      stage(0, true, true);
      const int pull_at = PULLM == 0 ? 99 : (PULLM == 1 ? gw + W2T_AT : ((gw & 1) ? 99 : W2T_AT + (gw >> 1) * W2T_SP));
      int l16 = lane * 16, w2row;
      {
        const int o = 448 * (gw & 1) + 2 * (xcd * 28 + j);
        w2row = 448 * ((o >> 5) & 3) + 64 * (o >> 7) + 32 * (1 - (gw >> 1)) + (o & 31);
      }
P1UNROLL
      for (int s = 1; s < 7; ++s) {
        asm volatile("s_cmp_lg_u32 %3, " XT_FLAG_AT "\n\ts_cbranch_scc1 1f\n\ts_cmp_lt_u32 %4, %3\n\ts_cbranch_scc1 2f\n\ts_waitcnt vmcnt(0)\n2:\n\ts_waitcnt vmcnt(2)\n\ts_mov_b64 exec, 1\n\tglobal_store_dwordx2 %0, %1, %2 sc1\n\ts_mov_b64 exec, -1\n1:"
                     :: "v"(f0o), "v"(tag2), "s"(ws), "s"(s), "s"(pull_at) : "memory", "scc");
        asm volatile("s_cmp_lg_u32 %4, %5\n\ts_cbranch_scc1 1f\n\ts_mov_b32 m0, %3\n\ts_nop 0\n\tbuffer_load_dwordx4 %0, %1, %2 offen lds\n\tbuffer_load_dwordx4 %0, %1, %2 offen offset:1024 lds\n1:"
                     :: "v"(l16), "s"(s2), "s"(w2row * 1024), "s"(XI_OFF2), "s"(s), "s"(pull_at) : "memory", "scc");
        stage(s, true);
      }
      stage(7, false);
      SCHED();
      SCHED();
      STB(10);
      STAMP(2);
#pragma unroll
      for (int a = 0; a < 2; ++a)
#pragma unroll
        for (int c = 0; c < 2; ++c) acc[a][c] = mfma32(Ap[a], Bp[c], acc[a][c]);
      STAMP(3);
      f2 c0v[2], c1v[2], sv[2];
#pragma unroll
      for (int c = 0; c < 2; ++c) {
        const float s = sd[c], rs = __builtin_amdgcn_rcpf(s);
        const float c0 = -2.3022081985f * rs, c1 = c0 * 0.044715f * rs * rs;
        c0v[c] = f2{c0, c0}; c1v[c] = f2{c1, c1}; sv[c] = f2{s, s};
      }
#pragma unroll
      for (int a = 0; a < 2; ++a) {
#pragma unroll
        for (int c = 0; c < 2; ++c)
#pragma unroll
          for (int u = 0; u < 2; ++u) {
            v4u o;
#pragma unroll
            for (int e = 0; e < 4; ++e) {
              f2 v = {acc[a][c][8 * u + 2 * e], acc[a][c][8 * u + 2 * e + 1]};
              f2 z = v * __builtin_elementwise_fma(v * v, c1v[c], c0v[c]);
              f2 ex = {__builtin_amdgcn_exp2f(z.x), __builtin_amdgcn_exp2f(z.y)};
              f2 d = __builtin_elementwise_fma(ex, sv[c], sv[c]);
              f2 r = {__builtin_amdgcn_rcpf(d.x), __builtin_amdgcn_rcpf(d.y)};
              f2 g = v * r;
              o[e] = pk(g.x, g.y);
            }
            __builtin_amdgcn_raw_buffer_store_b128(o, rw, lane * 16 + (WS_H + ((4 * pp + 2 * wl + a) * 112 + 8 * fs + 4 * wf + 2 * u + c) * 1024), 0, SC1);
          }
        SCHED();
      }
      asm volatile("s_mov_b64 exec, 1\n\tds_write_b32 %0, %1\n\ts_waitcnt vmcnt(4)\n\tglobal_store_dwordx2 %2, %3, %4 sc1\n\ts_mov_b64 exec, -1"
                   :: "v"(aD), "v"(aD), "v"(fha), "v"(tag2), "s"(ws) : "memory");
    } else {
      asm volatile("; spare" ::: "memory");
      late();
      const int job = (xcd & 3) * 4 + (j - 28), sp = (xcd >> 2) * 14 + min(job, 13);
      float cs[8];
#pragma unroll
      for (int i = 0; i < 8; ++i) cs[i] = 0.f;
      v4u v[16];
      BAR();
      __builtin_amdgcn_s_setprio(3);
      xt_in();
#pragma unroll
      for (int i = 0; i < 16; ++i) v[i] = LD128(S + gw * 16384 + i * 1024 + lane * 16);
      BAR();
      xt_out();
      SCHED();
      SCHED();
#pragma unroll
      for (int i = 0; i < 16; ++i)
#pragma unroll
        for (int e = 0; e < 4; ++e) {
          const unsigned w = v[i][e];
          asm("v_dot2c_f32_bf16 %0, %2, %3\n\tv_dot2c_f32_bf16 %1, %2, %4" : "+v"(cs[2 * e]), "+v"(cs[2 * e + 1]) : "v"(w), "v"(0x00003f80u), "v"(0x3f800000u));
        }
      asm volatile("s_nop 4" : "+v"(cs[0]), "+v"(cs[1]), "+v"(cs[2]), "+v"(cs[3]), "+v"(cs[4]), "+v"(cs[5]), "+v"(cs[6]), "+v"(cs[7]));
#pragma unroll
      for (int i = 0; i < 8; ++i) cs[i] = swap_sum32(swap_sum16(dpp_add<0x128>(cs[i])));
      if (lane < 8) {
        *(LAS f4*)(S + CSP_OFF + gw * 256 + lane * 32) = f4{cs[0], cs[1], cs[2], cs[3]};
        *(LAS f4*)(S + CSP_OFF + gw * 256 + lane * 32 + 16) = f4{cs[4], cs[5], cs[6], cs[7]};
      }
      VMW(0);
      if (lane == 0) st64(rw, WS_F0 + (gw * 256 + g8) * FS0, tlo, thi);
      STAMP(1);
      LGKM0();
      unsigned old = 0;
      if (lane == 0) old = __hip_atomic_fetch_add((LAS unsigned*)(S + CNT_OFF + 32), 1u, __ATOMIC_RELAXED, __HIP_MEMORY_SCOPE_WORKGROUP);
      asm volatile("" ::: "memory");
      if (job < 14 && __builtin_amdgcn_readfirstlane(old) == 3) {
        LAS float* c = (LAS float*)(S + CSP_OFF);
        float t = (c[lane] + c[64 + lane]) + (c[128 + lane] + c[192 + lane]);
        st64(rw, WS_CS + (sp * 64 + lane) * 8, __builtin_bit_cast(unsigned, t), tlo);
      }
      __builtin_amdgcn_s_setprio(0);
      STAMP(2);
      if (lane == 0) LD32V(S + CNT_OFF + 16 + gw * 4) = 1u;
      {
        const int w16 = (j - 28) * 4 + gw;
#pragma unroll 1
        for (int i = 0; i < 2 * PF_N; ++i)
          asm volatile("s_mov_b32 m0, %3\n\ts_nop 0\n\tbuffer_load_dwordx4 %0, %1, %2 offen lds\n\tbuffer_load_dwordx4 %0, %1, %2 offen offset:1024 lds\n\t"
                       "buffer_load_dwordx4 %0, %1, %2 offen offset:2048 lds\n\tbuffer_load_dwordx4 %0, %1, %2 offen offset:3072 lds"
                       :: "v"(lane * 16), "s"(s2), "s"((w16 >> 2) * 458752 + (6 + (((i >> 1) + xcd) & 7)) * 32768 + (w16 & 3) * 8192 + (i & 1) * 4096), "s"(XI_OFF + 2 * XI) : "memory");
        VMW(0);
      }
      STAMP(3);
    }
    STAMP(4);
    late();
    const int fho = WS_FH + ((pp * 2 + wl) * 64 + fs * 2 + wf) * FSH;
#pragma unroll 1
    for (int sp = 0; sp < SPIN && LD32V(S + CNT_OFF) < 4u; ++sp) __builtin_amdgcn_s_sleep(1);
    asm volatile("" ::: "memory");
#pragma unroll
    for (int n = 0; n < GPRE; ++n) dmaw(n);
    asm volatile("s_waitcnt vmcnt(%0)" :: "i"(GPRE + 1) : "memory");
    if (mainb && lane == 0) st64(rw, fho + 32 * FSH, tlo, thi);
    STAMP(5);
#pragma unroll
    for (int n = GPRE; n < 14; ++n) dmaw(n);
#pragma unroll 1
    for (int sp = 0; sp < SPIN && LD32V(S + CNT_OFF + 48 + gw * 4) == 0u; ++sp) __builtin_amdgcn_s_sleep(1);
    asm volatile("" ::: "memory");
    u64 fh;
#pragma unroll
    for (int n = 14; n < 28; ++n) {
      if (n == HPOLL) asm volatile("global_load_dwordx2 %0, %1, %2 sc1" : "=v"(fh) : "v"(fo), "s"(ws) : "memory");
      dmaw(n);
      if (n == 21) { if (HPOLL <= 21) VMW(9); else VMW(8); }
      if (n >= 21) { trw(2 * n - 42); trw(2 * n - 41); }
    }
    STAMP(6);
    asm volatile("s_waitcnt vmcnt(%1)" : "+v"(fh) : "i"(28 - HPOLL) : "memory");
    if (!tag_ok(fh, tlo, thi)) {
#pragma unroll 1
      for (int it = 0; it < SPIN; ++it) if (tag_ok(ld64(ws, fo), tlo, thi)) break;
    }
    ORDER();
    STAMP(8);
#pragma unroll
    for (int i = 0; i < 8; ++i) ldh(i);
    VMW(8);
#pragma unroll
    for (int i = 14; i < 28; ++i) trw(i);
    LGKM0();
    if (lane == 0) __hip_atomic_fetch_add((LAS unsigned*)(S + CNT_OFF + 12), 1u, __ATOMIC_RELAXED, __HIP_MEMORY_SCOPE_WORKGROUP);
#pragma unroll
    for (int i = 8; i < HW; ++i) ldh(i);
  } else {
    __builtin_amdgcn_s_setprio(3);
    const int sw = wave - 4, cc = sw >> 1, sh = sw & 1;
    int vx[4], v1[4], rot, sx0, s10;
    int kx = 128, kw = 229376;
    {
      const int ln = tid & 63, xc = b0 & 7, jj = b0 >> 3;
#pragma unroll
      for (int i = 0; i < 4; ++i) {
        vx[i] = (ln >> 3) * 1024 + (((ln & 7) ^ ((4 * (i & 1) + (ln >> 4)) & 7)) << 4) + i * 7168;
        v1[i] = (ln >> 4) * 3584 + (((ln & 15) ^ ((ln >> 4) << 2) ^ i) << 4) + i * 13312;
      }
      rot = KORD ? 0 : 2 * (xc & 3) + 4 * (xc >> 2);
      sx0 = (((xc & 3) * 4 + (jj & 3)) * 128 + 32 * q3) * 1024; s10 = 16 * q3 * 3584 + ((xc >> 2) * 7 + (jj >> 2)) * 256;
      asm volatile("" : "+v"(vx[0]), "+v"(vx[1]), "+v"(vx[2]), "+v"(vx[3]), "+v"(v1[0]), "+v"(v1[1]), "+v"(v1[2]), "+v"(v1[3]));
      asm volatile("" : "+s"(rot), "+s"(sx0), "+s"(s10));
      if (__builtin_expect(!mainb, 0)) {
#pragma unroll
        for (int i = 0; i < 4; ++i) vx[i] = v1[i] = (ln >> 3) * 3584 + (ln & 7) * 16 + i * 27648;
        rot = 0; kx = kw = 256 * 3584;
        sx0 = 32 * q3 * 3584 + ((xc >> 2) * 14 + min((xc & 3) * 4 + jj - 28, 13)) * 128; s10 = sx0 + 128 * 3584;
      }
      SCHED();
    }
    const v4u s1 = mk_srd(w1, DM * FF * 2), sx = mk_srd(mainb ? x : w1, SL * DM * 2 - (mainb ? 0 : 262144));
    int sox, sow;
    auto issue1 = [&](int s, int n) {
      const int i = n & 3;
      if (n == 0) { const int ks = (s + rot) & 7; sox = sx0 + ks * kx; sow = s10 + ks * kw; }
      SCHED();
      if (i == 0) asm volatile("s_mov_b32 m0, %3\n\ts_nop 0\n\tbuffer_load_dwordx4 %0, %1, %2 offen lds" :: "v"(n ? v1[0] : vx[0]), "s"(n ? s1 : sx), "s"(n ? sow : sox), "s"((s % 3) * 32768 + q3 * 4096 + n * 4096) : "memory");
      else asm volatile("buffer_load_dwordx4 %0, %1, %2 offen offset:%3 lds" :: "v"(n < 4 ? vx[i] : v1[i]), "s"(n < 4 ? sx : s1), "s"(n < 4 ? sox : sow), "i"(i * 1024) : "memory");
      SCHED();
    };
#pragma unroll
    for (int n = 0; n < 4; ++n) issue1(0, n);
    u64 ev; unsigned p_ar, p_ai, p_br, p_bi, p_cr, p_ci;
    auto ldp = [&]() {
    asm volatile("global_load_ushort %0, %7, %9\n\tglobal_load_ushort %1, %7, %10\n\tglobal_load_ushort %2, %7, %11\n\tglobal_load_ushort %3, %7, %12\n\t"
                 "global_load_ushort %4, %7, %13\n\tglobal_load_ushort %5, %7, %14\n\tglobal_load_dwordx2 %6, %8, %15 sc1"
                 : "=&v"(p_ar), "=&v"(p_ai), "=&v"(p_br), "=&v"(p_bi), "=&v"(p_cr), "=&v"(p_ci), "=&v"(ev)
                 : "v"((((b0 & 7) * 64 + (b0 >> 3) * 2) * NM + tid - 256) * 2), "v"(WS_E),
                   "s"(Are), "s"(Aim), "s"(Bre), "s"(Bim), "s"(Cre), "s"(Cim), "s"(ws) : "memory");
    };
    if (PPOS == 0) ldp();
    const unsigned ldt2 = *(const CAS unsigned*)(logdt + (b0 & 7) * 64 + (b0 >> 3) * 2), dsk2 = *(const CAS unsigned*)(Dskip + (b0 & 7) * 64 + (b0 >> 3) * 2);
    issue1(0, 4);
    late();
    const int ch = ch0 + cc;
    asm volatile("" : "+v"(aW), "+v"(v2o));
    aW2 = aW;
    issue1(0, 5);
    unsigned aB = TB_OFF + cc * TAB + r31 * TROW + hh * 16, aA, aZ, aX, aK, aC = CNT_OFF, one = 1u;
    asm volatile("" : "+v"(aB));
    issue1(0, 6);
    {
      const int t4 = 4 * (r31 & 7), bk = r31 >> 3;
      const unsigned pat = sh ? 0x11100000u : (bk == 0 ? 0x76543210u : (bk == 1 ? 0x43222100u : 0x22111000u));
      const unsigned bas = sh ? 0x19171513u : 0x100d0800u;
      aA = TB_OFF + cc * TAB + (32 + ((bas >> (8 * bk)) & 255) + ((pat >> t4) & 15)) * TROW + hh * 16;
    }
    asm volatile("" : "+v"(aA));
    issue1(0, 7);
    {
      const unsigned z1_ = XI_OFF + cc * XI + min(lane, 59) * 8, z0_ = KK_OFF + cc * KCH + 8 + ((lane >> 2) & 1) * KC + 4096 + (lane & 3) * 8;
      aZ = z0_ + (z1_ - z0_) * sh;
    }
    asm volatile("" : "+v"(aZ));
    STB(10);
    if (PPOS == 1) ldp();
    issue1(1, 0);
    aX = XI_OFF + (lane & 1) * XI + 480 + (64 * sw + (lane >> 1)) * 16;
    aK = KK_OFF + cc * KCH + 8 + (2044 - (256 * (4 * sh + 3) + 8 * r31 + 4 * hh)) * 2;
    asm volatile("" : "+v"(aX), "+v"(aK));
    issue1(1, 1);
    if (lane < 16) LD32(S + CNT_OFF + lane * 4) = 0u;
    issue1(1, 2);
    const float ldt = cc ? bf_hi(ldt2) : bf_lo(ldt2), dsk = cc ? bf_hi(dsk2) : bf_lo(dsk2);
    issue1(1, 3);
    unsigned pa0, pb, aG = CNT_OFF + 48 + sw * 4;
    {
      const int m = lane & 15, g = lane >> 4;
      pa0 = KK_OFF + cc * KCH + (1 - (m & 1)) * KC + 8 + (2046 - 16 * (16 * 7 + 2 * (4 * sh + 3) + 1) - 2 * (m >> 1) + 8 * g) * 2;
      asm volatile("" : "+v"(pa0));
      issue1(1, 4);
      pb = XI_OFF + cc * XI + (30 + 2 * m - 4 * (4 * sh + 3) - 2 + g) * 16;
      asm volatile("" : "+v"(pb), "+v"(aG));
    }
#pragma unroll
    for (int n = 5; n < 8; ++n) issue1(1, n);
    __builtin_amdgcn_s_setprio(PRIO_EARLY);
    STB(12);
    if (PPOS == 2) ldp();
    if (!mainb) VMW(0);
    if (PPOS) VMW(15); else VMW(8);
    STAMP(1);
    STB(1);
    BAR();
    if (!mainb) { __builtin_amdgcn_s_setprio(2); BAR(); }
    if (mainb) { issue1(2, 0); issue1(2, 1); }
    asm volatile("s_waitcnt vmcnt(%7)" : "+v"(p_ar), "+v"(p_ai), "+v"(p_br), "+v"(p_bi), "+v"(p_cr), "+v"(p_ci), "+v"(ev) : "i"(PPOS == 2 ? 2 : 10) : "memory");
    const float dtv = rbf(__builtin_amdgcn_exp2f(ldt * 1.4426950408889634f));
    float lr2 = (dtv * bf_lo(p_ar)) * 1.4426950408889634f, lrev = (dtv * bf_lo(p_ai)) * 0.15915494309189535f;
    asm volatile("" : "+v"(lr2), "+v"(lrev));
    if (mainb) issue1(2, 2);
    float dbr = dtv * bf_lo(p_br);
    asm("" : "+v"(dbr));
    const f2 cb = cm(f2{bf_lo(p_cr), -bf_lo(p_ci)}, conj(mkz(f2{dbr, dtv * bf_lo(p_bi)})));
    asm volatile("" :: "v"(cb.x));
    if (mainb) issue1(2, 3);
    const cz z8 = mkz(cexp_(lr2, lrev, 8.f)), z8c = conj(z8);
    asm volatile("" :: "v"(z8.n), "v"(z8.r));
    if (mainb) issue1(2, 4);
    const cz z1 = conj(mkz(cexp_(lr2, lrev, 1.f)));
    asm volatile("" :: "v"(z1.i), "v"(z1.r));
    if (mainb) issue1(2, 5);
    const cz z256 = conj(mkz(cexp_(lr2, lrev, 256.f)));
    asm volatile("" :: "v"(z256.i), "v"(z256.r));
    if (mainb) issue1(2, 6);
    const cz z2 = mkz(cm(f2{z1.r, z1.i}, z1)), z4 = mkz(cm(f2{z2.r, z2.i}, z2));
    asm volatile("" :: "v"(z4.n), "v"(z2.n));
    { u64 e = ev + 1; tlo = __builtin_amdgcn_readfirstlane((unsigned)e); thi = __builtin_amdgcn_readfirstlane((unsigned)(e >> 32)); }
    if (mainb) issue1(2, 7);
    f2 V = {1.f, 0.f}, U = cb, W = cb;
    unsigned tv[32], tu[27];
    auto put = [&](int r) {
      const bool last = r == 7 || r == 12 || r == 15 || r == 18 || r == 20 || r == 22 || r == 24;
      if (r >= 27) { tv[r] = pk(V.x, V.y); V = cm(V, z8); asm volatile("" :: "v"(tv[r]), "v"(V.x)); return; }
      if (last) { unsigned d; tu[r] = pk(U.x, U.y); step2(V, z8, W, z256, tv[r], d); U = W; }
      else step2(V, z8, U, r < 8 ? z1 : (r < 13 ? z2 : (r < 19 ? z4 : z8c)), tv[r], tu[r]);
      asm volatile("" :: "v"(tv[r]), "v"(tu[r]));
    };
    if (mainb) {
    VMW(8);
    STAMP(2);
    STB(2);
    BAR();
#pragma unroll
    for (int i = 0; i < 8; ++i) { issue1(3, i); if (i >= 2 && i < 7) put(i - 2); }
    VMW(8);
    STB(3);
    BAR();
#pragma unroll
    for (int i = 0; i < 8; ++i) { issue1(4, i); if (i >= 1 && i < 7) put(4 + i); }
    VMW(8);
    STB(4);
    BAR();
#pragma unroll
    for (int i = 0; i < 8; ++i) { issue1(5, i); if (i >= 2 && i < 7) put(9 + i); }
    dmaw(0); dmaw(1);
    VMW(10);
    STB(5);
    BAR();
#pragma unroll
    for (int i = 0; i < 8; ++i) { issue1(6, i); if (i >= 2 && i < 7) put(14 + i); }
    dmaw(2); dmaw(3);
    VMW(12);
    STB(6);
    BAR();
#pragma unroll
    for (int i = 0; i < 8; ++i) { issue1(7, i); if (i >= 2 && i < 7) put(19 + i); }
    dmaw(4); dmaw(5);
    VMW(12);
    } else {
#pragma unroll
      for (int r = 0; r < 26; ++r) {
        if (PULLS && r < 14) {
          const int o = PMAP == 0 ? xcd * 16 + (j - 28) * 4 + sw : (PMAP == 1 ? sw * 32 + xcd * 4 + (j - 28) : sw * 32 + (j - 28) * 8 + xcd);
          SCHED();
          (PULLS == 2 ? dma_sc1 : dma)(s2, lane * 16, (448 * (o >> 5) + 64 * (r % 7) + 32 * (1 - r / 7) + (o & 31)) * 1024, XI_OFF2);
          SCHED();
        }
        put(r);
        if (r >= SP_AT && r < SP_AT + 6) dmaw(r - SP_AT);
      }
    }
    const int goff = cc == 0 ? WS_ST + (pp * 128 + 64 * sh + lane) * 8 : WS_CS + (fs * 128 + 64 * sh + lane) * 8;
    const int foff = WS_F0 + ((xcd >> 1) * 256 + 64 * sw + lane) * FS0;
    u64 gr, f0; unsigned b1v;
    STB(7);
    BARM();
#pragma unroll
    for (int i = 0; i < 8; ++i) {
      if (i == POLL_AT) asm volatile("global_load_dwordx2 %0, %3, %6 sc1\n\tglobal_load_dwordx2 %1, %4, %6 sc1\n\tglobal_load_ushort %2, %5, %7"
                 : "=&v"(gr), "=&v"(f0), "=&v"(b1v) : "v"(goff), "v"(foff), "v"((fs * 128 + 64 * sh + lane) * 2), "s"(ws), "s"(b1) : "memory");
      dmaw(6 + i); if (i >= 1 && i < 7) put(25 + i);
    }
    VMW(13);
    STB(8);
    BARM();
    v4u xv[2];
    asm volatile("" : "=v"(xv[0]), "=v"(xv[1]));
    int xvo = (lane >> 1) * 8192 + (lane & 1) * 16;
    const int xso = WS_XT + 64 * sw * 8192 + ch0 * 16;
    unsigned aW1 = aW ^ 64;
    asm volatile("" : "+v"(xvo), "+v"(aW1));
    f16v kk;
    const bool odd4 = sh && hh && (r31 & 1);
    float dk = sh == 0 && lane == 0 ? dsk : 0.f;
    v2u zero2 = {0u, 0u};
    asm volatile("" : "+v"(aC), "+v"(one), "+v"(dk), "+v"(zero2));
    auto arrive = [&](int off) {
      asm volatile("s_waitcnt lgkmcnt(0)\n\ts_mov_b64 exec, 1\n\tds_add_u32 %0, %1 offset:%2\n\ts_mov_b64 exec, -1" :: "v"(aC), "v"(one), "i"(off) : "memory");
    };
    if (!mainb) VMW(13);
#pragma unroll
    for (int i = 0; i < 4; ++i) trw(i);
    SCHED();
    asm volatile("s_waitcnt vmcnt(%3)" : "+v"(gr), "+v"(f0), "+v"(b1v) : "i"(8 - POLL_AT + (R2E ? 4 : 0)) : "memory");
    if (mainb) {
      if (!__all((unsigned)(gr >> 32) == tlo)) {
#pragma unroll 1
        for (int sp = 0; sp < SPIN; ++sp) { gr = ld64(ws, goff); if (__all((unsigned)(gr >> 32) == tlo)) break; }
      }
      const unsigned pv = (unsigned)gr;
      if (cc == 0) {
        const unsigned nm = pv & 0xffffu;
        LD64(S + BP_OFF + (64 * sh + lane) * 8) = v2u{nm | (nm << 16), pv};
      } else {
        const float c = __builtin_bit_cast(float, pv), c_hi = rbf(c), r1 = c - c_hi, c_mid = rbf(r1), r2 = r1 - c_mid;
        LD64(S + AP_OFF + (64 * sh + lane) * 8) = v2u{pk(c_hi, c_mid), (pk(r2, r2) & 0xffffu) | (b1v << 16)};
      }
    }
    const u64 seen = __ballot((unsigned)f0 == tlo && (unsigned)(f0 >> 32) == thi);
#pragma unroll
    for (int i = 4; i < 10; ++i) trw(i);
#define LDX(br) asm volatile("s_cmp_eq_u64 %6, -1\n\t" br " 1f\n\tbuffer_load_dwordx4 %0, %2, %3, %4 offen sc1\n\tbuffer_load_dwordx4 %1, %2, %3, %5 offen sc1\n1:" \
                   : "+v"(xv[0]), "+v"(xv[1]) : "v"(xvo), "s"(sw_), "s"(xso), "s"(xso + 32 * 8192), "s"(seen) : "memory", "scc")
    STAMP(3);
    STB(9);
    BARM();
    __builtin_amdgcn_s_setprio(2);
    LGKM0();
    LDX("s_cbranch_scc0");
    asm volatile("s_mov_b32 m0, %0\n\ts_nop 0" :: "s"(TB_OFF + cc * TAB + sh * 256) : "memory");
#pragma unroll
    for (int r = 0; r < 32; ++r) {
      asm volatile("ds_write_addtid_b32 %0 offset:%1" :: "v"(tv[r]), "i"(r * TROW) : "memory");
      if (r < 27) asm volatile("ds_write_addtid_b32 %0 offset:%1" :: "v"(tu[r]), "i"((32 + r) * TROW) : "memory");
    }
    arrive(4);
    LD64V(LP(aZ)) = zero2;
    if (seen != ~0ull) {
#pragma unroll 1
      for (int it = 0; it < SPIN; ++it) if (tag_ok(ld64(ws, foff), tlo, thi)) break;
    }
    LDX("s_cbranch_scc1");
    if (R2E) VMW(10); else VMW(2);
#pragma unroll
    for (int i = 10; i < 14; ++i) trw(i);
    if (R2E) { dmaw(22, true); dmaw(23, true); } else { dmaw(14, true); dmaw(15, true); dmaw(16, true); dmaw(17, true); }
#pragma unroll 1
    for (int sp = 0; sp < SPIN && __builtin_amdgcn_readfirstlane(LD32V(LP(aC) + 4)) < 4u; ++sp) __builtin_amdgcn_s_sleep(1);
    LGKM0();
    STAMP(4);
    {
      v4u A[16], B[16];
#pragma unroll
      for (int s = 0; s < 6; ++s) { A[s] = LD128(LP(aA) + s * 32); B[s] = LD128(LP(aB) + s * 32); }
      SCHED();
#pragma unroll
      for (int s = 0; s < 16; ++s) {
        if (s + 6 < 16) { A[s + 6] = LD128(LP(aA) + (s + 6) * 32); B[s + 6] = LD128(LP(aB) + (s + 6) * 32); }
        if (s == 0) {
#pragma unroll
          for (int i = 0; i < 16; ++i) kk[i] = 0.f;
        }
        kk = mfma32(A[s], B[s], kk);
        if (s < (R2E ? 4 : 10)) dmaw((R2E ? 24 : 18) + s, true);
        SCHED();
      }
    }
    STAMP(5);
#pragma unroll
    for (int g = 0; g < 4; ++g) {
      float k0 = kk[4 * g], k1 = kk[4 * g + 1], k2 = kk[4 * g + 2], k3 = kk[4 * g + 3];
      if (g == 0) k0 += dk;
      k0 = odd4 ? k1 : k0;
      const unsigned e01 = pk(k3, k2), e23 = pk(k1, k0), e12 = pk(k2, k1);
      LAS char* p = LP(aK) + (3 - g) * 512;
      LD64V(p) = v2u{e01, e23};
      LD16(p + KC - 2) = (u16)e01; LD32V(p + KC) = e12; LD16(p + KC + 4) = (u16)(e23 >> 16);
      SCHED();
    }
    if (sh == 0 && lane == 0) LD16(S + KK_OFF + cc * KCH + 8 + KC + 4094) = 0;
    asm volatile("s_waitcnt vmcnt(%2)" : "+v"(xv[0]), "+v"(xv[1]) : "i"(R2E ? 6 : 14) : "memory");
    LD128(LP(aX)) = xv[0]; LD128(LP(aX) + 512) = xv[1];
    arrive(0);
    VMW(0);
#pragma unroll
    for (int i = 14; i < 28; ++i) trw(i);
    asm volatile("s_waitcnt lgkmcnt(0)\n\ts_mov_b64 exec, 1\n\tds_write_b32 %0, %1\n\ts_mov_b64 exec, -1" :: "v"(aG), "v"(one) : "memory");
#pragma unroll 1
    for (int sp = 0; sp < SPIN && __builtin_amdgcn_readfirstlane(LD32V(LP(aC))) < 4u; ++sp) __builtin_amdgcn_s_sleep(1);
    asm volatile("" ::: "memory");
    if (b == 0 && tid == 256) st64(rw, WS_E, tlo, thi);
    STAMP(6);
    u64 fh;
    unsigned aE = KK_OFF + cc * KCH + sh * 4096 + lane * 16, aF = KK_OFF + cc * KCH + (sh ^ 1) * 4096 + lane * 16, aP = CNT_OFF + 40 + cc * 4;
    int aY = WS_Y + ch * 4096 + (1024 * sh + 16 * (lane & 15) + 4 * (lane >> 4)) * 2;
    f4 ya[8];
#pragma unroll
    for (int i = 0; i < 8; ++i) ya[i] = f4{0.f, 0.f, 0.f, 0.f};
    {
      unsigned pa4[4];
#pragma unroll
      for (int i = 0; i < 4; ++i) { pa4[i] = pa0 + 1024 * i; asm volatile("" : "+v"(pa4[i])); }
      v4u A[4][8], B[4][8];
      auto lda = [&](int q, int p) { LAS unsigned* a = (LAS unsigned*)(LP(pa4[(7 - p) >> 1]) + 512 * ((7 - p) & 1) + 64 * (3 - q)); A[q][p] = v4u{a[0], a[1], a[2], a[3]}; };
      auto ldb = [&](int q, int e) { B[q][e] = *(volatile LAS v4u*)(LP(pb) + 512 * e + 64 * (3 - q)); };
      lda(0, 0);
#pragma unroll
      for (int e = 7; e >= 0; --e) ldb(0, e);
      lda(0, 1); lda(0, 2);
#pragma unroll 1
      for (int sp = 0; sp < SPIN && __builtin_amdgcn_readfirstlane(LD32V(LP(aG) - 32)) == 0u; ++sp) __builtin_amdgcn_s_sleep(1);
      STAMP(13);
      SCHED();
      sfor<4>([&](auto q_) __attribute__((always_inline)) {
        constexpr int q = q_.v;
        if (q == 1) asm volatile("" : "+v"(aE), "+v"(aF), "+v"(aP), "+v"(aY));
        if (q == 3) asm volatile("global_load_dwordx2 %0, %1, %2 sc1" : "=v"(fh) : "v"(fo), "s"(ws) : "memory");
        int n = 0;
#pragma unroll
        for (int p = 0; p < 8; ++p)
#pragma unroll
          for (int e = 7 - p; e >= 0; --e) {
            ya[p + e] = mfma16(A[q][p], B[q][e], ya[p + e]);
            if (n == 2) lda(q, 3);
            if (n == 8) lda(q, 4);
            if (n == 12) lda(q, 5);
            if (n == 16) lda(q, 6);
            if (n == 18) lda(q, 7);
            if (q == 3 && n == 28) asm volatile("s_waitcnt lgkmcnt(0)\n\ts_mov_b64 exec, 1\n\tds_add_u32 %0, %1\n\ts_mov_b64 exec, -1" :: "v"(aP), "v"(one) : "memory");
            if constexpr (q < 3) {
              if (n == 20) lda(q + 1, 0);
              if (n == 28) lda(q + 1, 1);
              if (n == 34) lda(q + 1, 2);
              if (n == 4) ldb(q + 1, 7);
              if (n == 10) ldb(q + 1, 6);
              if (n == 17) ldb(q + 1, 5);
              if (n == 22) ldb(q + 1, 4);
              if (n == 24) ldb(q + 1, 3);
              if (n == 26) ldb(q + 1, 2);
              if (n == 30) ldb(q + 1, 1);
              if (n == 31) ldb(q + 1, 0);
            }
            SCHED();
            ++n;
          }
      });
    }
    STAMP(7);
#pragma unroll 1
    for (int sp = 0; sp < SPIN && __builtin_amdgcn_readfirstlane(LD32V(LP(aP))) < 2u; ++sp) {}
    asm volatile("" ::: "memory");
    auto finish = [&](int own, int oth) {
#pragma unroll
      for (int i = 0; i < 4; ++i) *(LAS f4*)(LP(aE) + i * 1024) = ya[oth + i];
      asm volatile("s_waitcnt lgkmcnt(0)\n\ts_mov_b64 exec, 1\n\tds_add_u32 %0, %1\n\ts_mov_b64 exec, -1" :: "v"(aP), "v"(one) : "memory");
#pragma unroll 1
      for (int sp = 0; sp < SPIN && __builtin_amdgcn_readfirstlane(LD32V(LP(aP))) < 4u; ++sp) {}
      asm volatile("" ::: "memory");
      f4 y[4];
#pragma unroll
      for (int i = 0; i < 4; ++i) y[i] = *(LAS f4*)(LP(aF) + i * 1024);
#pragma unroll
      for (int i = 0; i < 4; ++i) {
#pragma unroll
        for (int e = 0; e < 4; ++e) { float t = y[i][e]; asm("v_add_f32 %0, %0, %1" : "+v"(t) : "v"(ya[own + i][e])); y[i][e] = t; }
        st64(rw, aY + i * 512, pk(y[i][0], y[i][1]), pk(y[i][2], y[i][3]));
      }
    };
    if (sh) finish(4, 0); else finish(0, 4);
    arrive(12);
    asm volatile("s_waitcnt vmcnt(4)" : "+v"(fh) :: "memory");
    if (!tag_ok(fh, tlo, thi)) {
#pragma unroll 1
      for (int it = 0; it < SPIN; ++it) if (tag_ok(ld64(ws, fo), tlo, thi)) break;
    }
    ORDER();
    STAMP(8);
#pragma unroll
    for (int i = 0; i < YF; ++i) ldh(i);
    asm volatile("s_waitcnt vmcnt(%0)" :: "i"(YF) : "memory");
    if (lane == 0) st64(rw, WS_FY + (sh * 512 + ch) * FSY, tlo, thi);
    __builtin_amdgcn_s_setprio(0);
#pragma unroll
    for (int i = YF; i < HW; ++i) ldh(i);
  }
  STAMP(9);
  late();
  f16v acc[2][2];
#pragma unroll
  for (int c = 0; c < 4; ++c)
#pragma unroll
    for (int i = 0; i < 16; ++i) acc[c >> 1][c & 1][i] = 0.f;
  unsigned oo = (64 * lt + 8 * wave + (lane >> 3)) * 1024 + dt * 128 + (lane & 7) * 16;
  unsigned p0, p1, p2, p3, cnt, rp8[8];
  const v4u xo = *(const GAS v4u*)((const GAS char*)x + oo), bo = *(const GAS v4u*)((const GAS char*)b2 + dt * 128 + (lane & 7) * 16);
  f2 xb[4];
  STAMP(10);
#pragma unroll
  for (int k = 0; k < 14; ++k) {
#pragma unroll
    for (int c = 0; c < 2; ++c) {
      acc[0][c] = mfma32(w2f[2 * k], H[2 * k + c], acc[0][c]);
      acc[1][c] = mfma32(w2f[2 * k + 1], H[2 * k + c], acc[1][c]);
      if (2 * k + c + HW < 28) ldh(2 * k + c + HW);
      SCHED();
    }
    if (k == 9) {
      p0 = (8 * wave + (lane >> 3)) * 256 + (((lane >> 3) & 1) << 7) + (((lane ^ (lane >> 3)) & 7) << 4); p1 = p0 ^ 128;
      p2 = 4 * RD_W + (8 * wave + (lane >> 3)) * 128 + (((lane ^ (lane >> 3)) & 7) << 4);
      p3 = Y2_OFF + (8 * wave + (lane >> 3)) * Y2_ROW + (lane & 7) * 16;
      asm volatile("" : "+v"(p0), "+v"(p1), "+v"(p2), "+v"(p3), "+v"(oo));
      SCHED();
    }
    if (k == 12) {
      cnt = LD32V(S + CNT_OFF + 12);
      const unsigned rp = dh ? 4 * RD_W + q3 * 8192 + r31 * 128 + ((r31 & 7) << 4) + hh * 8 : q3 * RD_W + r31 * 256 + ((hh ^ (r31 & 1)) << 7) + ((r31 & 7) << 4);
#pragma unroll
      for (int p = 0; p < 8; ++p) { rp8[p] = rp ^ (p << 4); asm volatile("" : "+v"(rp8[p])); }
      SCHED();
    }
    if (k == 10 || k == 11) {
#pragma unroll
      for (int e = 2 * (k - 10); e < 2 * (k - 9); ++e) {
        float lo = bf_lo(xo[e]), hi = bf_hi(xo[e]);
        asm volatile("v_add_f32 %0, %0, %2\n\tv_add_f32 %1, %1, %3" : "+v"(lo), "+v"(hi) : "v"(bf_lo(bo[e])), "v"(bf_hi(bo[e])));
        xb[e] = f2{lo, hi};
      }
      SCHED();
    }
  }
  STAMP(11);
  if (__builtin_amdgcn_readfirstlane(cnt) < 8u) {
#pragma unroll 1
    for (int sp = 0; sp < SPIN && LD32V(S + CNT_OFF + 12) < 8u; ++sp) __builtin_amdgcn_s_sleep(1);
  }
  asm volatile("" ::: "memory");
  if (wave < 4) {
    const int fyo = WS_FY + ((lt >> 4) * 512 + 64 * dt + 16 * wave + (lane & 15)) * FSY;
    const u64 fy = ld64(ws, fyo);
    SCHED();
    auto slab = [&](int p) {
#pragma unroll
      for (int c = 0; c < 2; ++c) {
        const f16v& a = acc[p >> 2][c];
        const int g = p & 3;
        *(LAS f4*)(LP(rp8[p]) + c * 8192) = f4{a[4 * g], a[4 * g + 1], a[4 * g + 2], a[4 * g + 3]};
      }
    };
#pragma unroll
    for (int p = 0; p < 5; ++p) slab(p);
    SCHED();
    if (!tag_ok(fy, tlo, thi)) {
#pragma unroll 1
      for (int it = 0; it < SPIN; ++it) if (tag_ok(ld64(ws, fyo), tlo, thi)) break;
    }
#pragma unroll
    for (int u = 0; u < 2; ++u) dma_sc1(sw_, (lane >> 3) * 4096 + (lane & 7) * 16, WS_Y + (64 * dt + 16 * wave + 8 * u) * 4096 + lt * 128, YI_OFF + (2 * wave + u) * 1024);
#pragma unroll
    for (int p = 5; p < 8; ++p) slab(p);
    VMW(0);
#pragma unroll
    for (int u = 0; u < 2; ++u) {
      LAS char* p = S + YI_OFF + (2 * wave + u) * 1024 + trq * 128 + ((lane & 48) + 4 * trp) * 2;
      LD128(S + Y2_OFF + lane * Y2_ROW + (2 * wave + u) * 16) = cat(trd(p), trd(p + 512));
    }
    STAMP(7);
  } else {
    __builtin_amdgcn_s_setprio(2);
#pragma unroll
    for (int c = 0; c < 2; ++c)
#pragma unroll
      for (int p = 0; p < 8; ++p) {
        const f16v& a = acc[p >> 2][c];
        const int g = p & 3;
        LD64V(LP(rp8[p]) + c * 4096) = v2u{pk(a[4 * g], a[4 * g + 1]), pk(a[4 * g + 2], a[4 * g + 3])};
      }
    __builtin_amdgcn_s_setprio(0);
  }
  BAR();
  STAMP(12);
  {
    f4 s0[4], s1[4];
    v4u w[5];
#pragma unroll
    for (int q = 0; q < 4; ++q) w[q] = LD128(LP(p2) + q * 8192);
    w[4] = LD128(LP(p3));
#pragma unroll
    for (int q = 0; q < 4; ++q) { s0[q] = *(LAS f4*)(LP(p0) + q * RD_W); s1[q] = *(LAS f4*)(LP(p1) + q * RD_W); }
    SCHED();
    v4u o;
#pragma unroll
    for (int e = 0; e < 4; ++e) {
      f2 m = xb[e];
#pragma unroll
      for (int q = 0; q < 5; ++q) m += f2{bf_lo(w[q][e]), bf_hi(w[q][e])};
#pragma unroll
      for (int q = 0; q < 4; ++q) m += e < 2 ? f2{s0[q][2 * e], s0[q][2 * e + 1]} : f2{s1[q][2 * e - 4], s1[q][2 * e - 3]};
      o[e] = pk(m.x, m.y);
    }
    __builtin_amdgcn_raw_buffer_store_b128(o, mk_rsrc(out, SL * DM * 2), oo, 0, SC1);
  }
  asm volatile(".Lfk_end:" ::: "memory");
}

extern "C" void kernel_launch(void* const* d_in, const int* in_sizes, int n_in, void* d_out, int out_size, void* d_ws, size_t ws_size,
                              hipStream_t stream) {
  (void)hipFuncSetAttribute((const void*)fused_k, hipFuncAttributeMaxDynamicSharedMemorySize, LDS_BYTES);
  fused_k<<<256, 512, LDS_BYTES, stream>>>((const u16*)d_in[0], (const u16*)d_in[1], (const u16*)d_in[2], (const u16*)d_in[3], (const u16*)d_in[4],
                                           (const u16*)d_in[5], (const u16*)d_in[6], (const u16*)d_in[7], (const u16*)d_in[8], (const u16*)d_in[10],
                                           (const u16*)d_in[11], (const u16*)d_in[12], (const u16*)d_in[13], (u16*)d_out, (char*)d_ws);
}
```
